```python
import torch

def _softmax(x, axis=-1):
    return torch.softmax(x, dim=axis)
def _gelu(x):
    return torch.nn.functional.gelu(x, approximate="tanh")
def _silu(x):
    return torch.nn.functional.silu(x)
def _sigmoid(x):
    return torch.sigmoid(x)
def _layernorm(x, axis=-1, eps=1e-5):
    m = x.mean(dim=axis, keepdim=True)
    v = x.var(dim=axis, keepdim=True, unbiased=False)
    return (x - m) / torch.sqrt(v + eps)
def _scatter_add(tgt, idx, src, axis=0):
    return tgt.clone().index_add_(axis, idx, src)
def _segment_sum(src, seg, n):
    out = torch.zeros((n,) + src.shape[1:], dtype=src.dtype)
    return out.index_add_(0, seg, src)
def _pad(x, pw):
    flat = tuple(v for pair in reversed(pw) for v in pair)
    return torch.nn.functional.pad(x, flat)

kernel_name = 'recurrent_scan__sliding_block_cached__dense_activation_relu__decode'

def setup_inputs():

    _w = (lambda a: (lambda t: t.to(torch.bfloat16) if t.is_floating_point() else t)(torch.as_tensor(a)))
    def _A_setup():
        B, Sq, D = 2, 1, 1024
        Hq, Hkv, Sk, Dh = 32, 4, 32768, 128
        assert Hq % Hkv == 0, (Hq, Hkv)
        return {
            "x": _w((lambda *s: torch.randn(*s, dtype=torch.bfloat16) * 0.05)(B, Sq, D)),
            "k_cache": _w((lambda *s: torch.randn(*s, dtype=torch.bfloat16) * 1.6)(B, Hkv, Sk, Dh)),
            "v_cache": _w((lambda *s: torch.randn(*s, dtype=torch.bfloat16) * 0.2)(B, Hkv, Sk, Dh)),
            "wq": _w((lambda *s: torch.randn(*s, dtype=torch.bfloat16) * 1.6)(D, Hq * Dh) * (D ** -0.5)),
            "wo": _w((lambda *s: torch.randn(*s, dtype=torch.bfloat16) * 0.2)(Hq * Dh, D) * ((Hq * Dh) ** -0.5)),
        }
    _A_in = _A_setup()

    def _B_setup():
        B, S, D, Dff = 2, 1, 1024, 3584
        return {
            "x": _w((lambda *s: torch.randn(*s, dtype=torch.bfloat16) * 0.05)(B, S, D)),
            "w1": _w((lambda *s: torch.randn(*s, dtype=torch.bfloat16) * 0.1)(D, Dff)),
            "w2": _w((lambda *s: torch.randn(*s, dtype=torch.bfloat16) * 0.1)(Dff, D)),
        }
    _B_in = _B_setup()

    return {
        "A_x": _A_in["x"],
        "A_k_cache": _A_in["k_cache"],
        "A_v_cache": _A_in["v_cache"],
        "A_wq": _A_in["wq"],
        "A_wo": _A_in["wo"],
        "B_x": _B_in["x"],
        "B_w1": _B_in["w1"],
        "B_w2": _B_in["w2"],
    }

def reference(A_x, A_k_cache, A_v_cache, A_wq, A_wo, B_x, B_w1, B_w2):
    def _A_ref(x, k_cache, v_cache, wq, wo):
        B, Sq, D = x.shape
        Hq, Dh = 32, 128
        Hkv, Sk = k_cache.shape[1], k_cache.shape[2]
        W = min(4096, max(256, Sk // 4))
        rep = Hq // Hkv
        q = (lambda x, *d: x.permute(*d))((x @ wq).reshape(B, Sq, Hq, Dh), 0, 2, 1, 3)
        k = (lambda x, n, axis: torch.repeat_interleave(x, n, dim=axis))(k_cache[:, :, Sk - W:, :], rep, axis=1)
        v = (lambda x, n, axis: torch.repeat_interleave(x, n, dim=axis))(v_cache[:, :, Sk - W:, :], rep, axis=1)
        scores = torch.einsum("bhqd,bhkd->bhqk", q, k) * (Dh ** -0.5)
        p = _softmax(scores, axis=-1)
        o = torch.einsum("bhqk,bhkd->bhqd", p, v)
        return (lambda x, *d: x.permute(*d))(o, 0, 2, 1, 3).reshape(B, Sq, Hq * Dh) @ wo

    def _B_ref(x, w1, w2):
        h = torch.einsum("bsd,df->bsf", x, w1)
        zero = torch.zeros_like(h)
        h = torch.maximum(h, zero)
        return torch.einsum("bsf,fd->bsd", h, w2)

    x = A_x
    xn = _layernorm(x, axis=-1)
    s = _A_ref(xn, A_k_cache, A_v_cache, A_wq, A_wo)
    g = _silu(_B_ref(xn, B_w1, B_w2))
    return x + s * g
```

```cpp
#include <hip/hip_runtime.h>
#include <hip/hip_bf16.h>
#include <stdint.h>

typedef unsigned short u16;
__device__ __forceinline__ float bf2f(u16 b) { return __uint_as_float(((unsigned)b) << 16); }
__device__ __forceinline__ float rbf(float f) { return __bfloat162float(__float2bfloat16(f)); }
__device__ __forceinline__ u16 f2bf(float f) { __hip_bfloat16 h = __float2bfloat16(f); return *reinterpret_cast<u16*>(&h); }

constexpr int D = 1024, HQ = 32, HKV = 4, SK = 32768, DH = 128, W = 4096, DFF = 3584;

__device__ void compute_xn(const u16* x, float* xn  , float* red) {
    int t = threadIdx.x;
    for (int b = 0; b < 2; ++b) {
        float v[4]; float s = 0;
        for (int i = 0; i < 4; ++i) { v[i] = bf2f(x[b * D + t * 4 + i]); s += v[i]; }
        for (int o = 32; o; o >>= 1) s += __shfl_xor(s, o);
        __syncthreads();
        if ((t & 63) == 0) red[t >> 6] = s;
        __syncthreads();
        float mean = (red[0] + red[1] + red[2] + red[3]) * (1.0f / D);
        float q = 0;
        for (int i = 0; i < 4; ++i) { float d = v[i] - mean; q += d * d; }
        for (int o = 32; o; o >>= 1) q += __shfl_xor(q, o);
        __syncthreads();
        if ((t & 63) == 0) red[t >> 6] = q;
        __syncthreads();
        float var = (red[0] + red[1] + red[2] + red[3]) * (1.0f / D);
        float mb = rbf(mean), vb = rbf(var);
        float den = rbf(sqrtf(rbf(vb + 1e-5f)));
        for (int i = 0; i < 4; ++i) xn[b * D + t * 4 + i] = rbf(rbf(v[i] - mb) / den);
    }
    __syncthreads();
}

__global__ void k1(const u16* x, const u16* wq, const u16* w1, float* q, float* h) {
    __shared__ float xn[2 * D]; __shared__ float red[4];
    compute_xn(x, xn, red);
    int c = blockIdx.x * 256 + threadIdx.x;
    const u16* w; int ld; float* o; int n; bool relu;
    if (c < 4096) { w = wq + c; ld = 4096; o = q + c; n = 4096; relu = false; }
    else { c -= 4096; w = w1 + c; ld = DFF; o = h + c; n = DFF; relu = true; }
    float a0 = 0, a1 = 0;
    for (int k = 0; k < D; ++k) { float wv = bf2f(w[(size_t)k * ld]); a0 += xn[k] * wv; a1 += xn[D + k] * wv; }
    a0 = rbf(a0); a1 = rbf(a1);
    if (relu) { a0 = fmaxf(a0, 0.f); a1 = fmaxf(a1, 0.f); }
    o[0] = a0; o[n] = a1;
}
__global__ void k2(const float* q, const u16* kc, const u16* vc, float* o) {
    __shared__ float sc[W]; __shared__ float qs[DH]; __shared__ float red[4];
    int b = blockIdx.x / HQ, hh = blockIdx.x % HQ, kvh = hh / 8, t = threadIdx.x;
    if (t < DH) qs[t] = q[b * 4096 + hh * DH + t];
    __syncthreads();
    const u16* kp = kc + ((size_t)(b * HKV + kvh) * SK + (SK - W)) * DH;
    const u16* vp = vc + ((size_t)(b * HKV + kvh) * SK + (SK - W)) * DH;
    float mx = -INFINITY;
    for (int j = t; j < W; j += 256) {
        float a = 0;
        for (int d = 0; d < DH; ++d) a += qs[d] * bf2f(kp[(size_t)j * DH + d]);
        a = rbf(rbf(a) * 0.08838834764831845f);
        sc[j] = a; mx = fmaxf(mx, a);
    }
    for (int of = 32; of; of >>= 1) mx = fmaxf(mx, __shfl_xor(mx, of));
    if ((t & 63) == 0) red[t >> 6] = mx;
    __syncthreads();
    mx = fmaxf(fmaxf(red[0], red[1]), fmaxf(red[2], red[3]));
    __syncthreads();
    float s = 0;
    for (int j = t; j < W; j += 256) { float e = expf(sc[j] - mx); sc[j] = e; s += e; }
    for (int of = 32; of; of >>= 1) s += __shfl_xor(s, of);
    if ((t & 63) == 0) red[t >> 6] = s;
    __syncthreads();
    s = red[0] + red[1] + red[2] + red[3];
    if (t < DH) {
        float a = 0;
        for (int j = 0; j < W; ++j) a += rbf(sc[j] / s) * bf2f(vp[(size_t)j * DH + t]);
        o[b * 4096 + hh * DH + t] = rbf(a);
    }
}
__global__ void k3(const u16* x, const float* o, const float* h, const u16* wo, const u16* w2, u16* out) {
    int c = blockIdx.x * 64 + threadIdx.x; int b = c / D; c %= D;
    float s = 0, f = 0;
    for (int k = 0; k < 4096; ++k) s += o[b * 4096 + k] * bf2f(wo[(size_t)k * D + c]);
    for (int k = 0; k < DFF; ++k) f += h[b * DFF + k] * bf2f(w2[(size_t)k * D + c]);
    s = rbf(s); f = rbf(f);
    float g = rbf(f / (1.f + expf(-f)));
    out[b * D + c] = f2bf(bf2f(x[b * D + c]) + rbf(s * g));
}

extern "C" void kernel_launch(void* const* d_in, const int* in_sizes, int n_in,
                              void* d_out, int out_size, void* d_ws, size_t ws_size,
                              hipStream_t stream) {
    const u16* x = (const u16*)d_in[0]; const u16* kc = (const u16*)d_in[1]; const u16* vc = (const u16*)d_in[2];
    const u16* wq = (const u16*)d_in[3]; const u16* wo = (const u16*)d_in[4];
    const u16* w1 = (const u16*)d_in[6]; const u16* w2 = (const u16*)d_in[7];
    float* q = (float*)d_ws; float* h = q + 8192; float* o = h + 8192;
    k1<<<30, 256, 0, stream>>>(x, wq, w1, q, h);
    k2<<<64, 256, 0, stream>>>(q, kc, vc, o);
    k3<<<32, 64, 0, stream>>>(x, o, h, wo, w2, (u16*)d_out);
}
```

```cpp
#include <hip/hip_runtime.h>
#include <hip/hip_bf16.h>
#include <stdint.h>

typedef unsigned v4u __attribute__((ext_vector_type(4)));
typedef unsigned v2u __attribute__((ext_vector_type(2)));
typedef float v4f __attribute__((ext_vector_type(4)));
typedef __bf16 v8bf __attribute__((ext_vector_type(8)));
typedef unsigned v16u __attribute__((ext_vector_type(16)));
typedef unsigned v8u __attribute__((ext_vector_type(8)));
typedef unsigned long long u64;
typedef __amdgpu_buffer_rsrc_t rsrc_t;

#define HSLEEP 1
#define EARLY 2
#define SB() __builtin_amdgcn_sched_barrier(0)
#define BAR() do { SB(); __builtin_amdgcn_s_barrier(); SB(); } while (0)
#define CB() asm volatile("" ::: "memory")
#define VMCNT(n) do { SB(); asm volatile("s_waitcnt vmcnt(%0)" :: "n"(n) : "memory"); SB(); } while (0)

constexpr int SK = 32768, WIN_KEYS = 4096;
constexpr int NT = 2, SC1 = 16;
constexpr int OOB = 0x40000000;
constexpr int SPIN = 1 << 22;
constexpr int WS_EP = 0, FL = 4352, WS_QF = 0x210000, WS_OF = WS_QF + 32 * FL, WS_HF = WS_OF + 32 * FL, WS_FF = WS_HF + 32 * FL;
constexpr int WS_QP = 0x10000, WS_HP = 0x50000, WS_ML = 0x88000, WS_FP = 0x90000, WS_OP = 0x110000, WS_END = WS_FF + 32 * FL;
constexpr int HSPIN = 200;
enum { C_ARR = 0, C_X = 1, C_HINT = 2, C_Q = 3, C_M = 4, C_O = 5, C_H = 6, C_T = 7, C_P = 8, C_D = 9 };
constexpr int TS = 36;

struct alignas(16) Lds {
    unsigned ctl[16];
    unsigned tx[8 * TS], th[8 * TS], to[8 * TS];
    unsigned qt[512];
    float ml[8][4][2];
    float ow[4][1024];
};

__device__ __forceinline__ rsrc_t mk(const void* p, int n) { return __builtin_amdgcn_make_buffer_rsrc((void*)p, 0, n, 0x00020000); }
template <int AUX> __device__ __forceinline__ v4u ld4(rsrc_t r, int off) { return __builtin_amdgcn_raw_buffer_load_b128(r, off, 0, AUX); }
__device__ __forceinline__ void st4(rsrc_t r, int off, v4u v) { __builtin_amdgcn_raw_buffer_store_b128(v, r, off, 0, SC1); }
__device__ __forceinline__ void st1(rsrc_t r, int off, unsigned v) { __builtin_amdgcn_raw_buffer_store_b32(v, r, off, 0, SC1); }
__device__ __forceinline__ float u2f(unsigned u) { return __uint_as_float(u); }
__device__ __forceinline__ unsigned f2u(float f) { return __float_as_uint(f); }
__device__ __forceinline__ unsigned pk_bf(float lo, float hi) { unsigned r; asm("v_cvt_pk_bf16_f32 %0, %1, %2" : "=v"(r) : "v"(lo), "v"(hi)); return r; }
__device__ __forceinline__ float rbf(float f) { return u2f(pk_bf(f, f) & 0xffff0000u); }
__device__ __forceinline__ float blo(unsigned u) { return u2f(u << 16); }
__device__ __forceinline__ float bhi(unsigned u) { return u2f(u & 0xffff0000u); }
template <int CTRL> __device__ __forceinline__ float dpp(float v) { return u2f(__builtin_amdgcn_update_dpp(0u, f2u(v), CTRL, 0xf, 0xf, true)); }
__device__ __forceinline__ v4f mfma(v4u a, v4u b, v4f c) { return __builtin_amdgcn_mfma_f32_16x16x32_bf16(__builtin_bit_cast(v8bf, a), __builtin_bit_cast(v8bf, b), c, 0, 0, 0); }
__device__ __forceinline__ float swap32_add(float a, float b) { auto r = __builtin_amdgcn_permlane32_swap(f2u(a), f2u(b), false, false); return u2f(r[0]) + u2f(r[1]); }
__device__ __forceinline__ float swap16_add(float a, float b) { auto r = __builtin_amdgcn_permlane16_swap(f2u(a), f2u(b), false, false); return u2f(r[0]) + u2f(r[1]); }
__device__ __forceinline__ float swap32_max(float a) { auto r = __builtin_amdgcn_permlane32_swap(f2u(a), f2u(a), false, false); return fmaxf(u2f(r[0]), u2f(r[1])); }
__device__ __forceinline__ float swap16_max(float a) { auto r = __builtin_amdgcn_permlane16_swap(f2u(a), f2u(a), false, false); return fmaxf(u2f(r[0]), u2f(r[1])); }
constexpr int XOR1 = 0xB1, XOR2 = 0x4E, HMIR = 0x141, ROR8 = 0x128, ROR4 = 0x124, ROR2 = 0x122, BCAST = 0x150;

__device__ __forceinline__ unsigned lds_get(unsigned* p) { return __builtin_amdgcn_readfirstlane(__hip_atomic_load(p, __ATOMIC_RELAXED, __HIP_MEMORY_SCOPE_WORKGROUP)); }
__device__ __forceinline__ void lds_wait_ge(unsigned* p, unsigned v) {
    CB();
#pragma unroll 1
    for (int it = 0; it < SPIN; ++it) { if (lds_get(p) >= v) break; __builtin_amdgcn_s_sleep(1); }
    CB();
}
__device__ __forceinline__ void lds_max(unsigned* p, unsigned v, int lane) { CB(); if (lane == 0) __hip_atomic_fetch_max(p, v, __ATOMIC_RELAXED, __HIP_MEMORY_SCOPE_WORKGROUP); CB(); }
__device__ __forceinline__ void lds_add(unsigned* p, unsigned v, int lane) { CB(); if (lane == 0) __hip_atomic_fetch_add(p, v, __ATOMIC_RELAXED, __HIP_MEMORY_SCOPE_WORKGROUP); CB(); }
__device__ __forceinline__ unsigned vm_low() { return __builtin_amdgcn_s_getreg(7 | (0 << 6) | (3 << 11)); }

template <int NW> __device__ __forceinline__ void hint_wait(const char* a, unsigned T) {
    const unsigned t4 = (T & 0xffu) * 0x01010101u;
#pragma unroll 1
    for (int it = 0; it < HSPIN; ++it) {
        v16u f; asm volatile("s_load_dwordx16 %0, %1, 0x0 glc\n\ts_waitcnt lgkmcnt(0)" : "=s"(f) : "s"(a) : "memory");
        unsigned d = 0;
#pragma unroll
        for (int i = 0; i < NW; ++i) d |= f[i] ^ t4;
        if (d == 0) break;
        __builtin_amdgcn_s_sleep(HSLEEP);
    }
}
__device__ __forceinline__ void stb(rsrc_t r, int off, unsigned v) { __builtin_amdgcn_raw_buffer_store_b8((unsigned char)v, r, off, 0, SC1); }
__device__ __forceinline__ v4u tag4(v4f v, unsigned T) {
    v4u r;
#pragma unroll
    for (int i = 0; i < 4; ++i) r[i] = (f2u(v[i]) & 0xffffff00u) | ((T >> (8 * i)) & 0xffu);
    return r;
}
__device__ __forceinline__ v4f untag4(v4u d, unsigned T, unsigned& bad) {
    v4f r;
#pragma unroll
    for (int i = 0; i < 4; ++i) { unsigned t = d[i] ^ ((T >> (8 * i)) & 0xffu); bad |= t; r[i] = u2f(t); }
    return r;
}
__device__ __forceinline__ bool all_ok(unsigned bad) { return __builtin_amdgcn_ballot_w64((bad & 0xffu) != 0) == 0; }

#define ST(i)

__device__ __forceinline__ void load_tab(const unsigned* tab, int lane, unsigned (&xa)[32]) {
    const v4u* t = (const v4u*)(tab + (((lane >> 3) & 1) * 4 + (lane >> 4)) * TS);
#pragma unroll
    for (int v = 0; v < 8; ++v) { v4u q = t[v]; xa[4 * v] = q[0]; xa[4 * v + 1] = q[1]; xa[4 * v + 2] = q[2]; xa[4 * v + 3] = q[3]; }
}
__device__ __forceinline__ v4u diag_mask(int lane) {
    int e = lane & 7; unsigned h = (e & 1) ? 0xffff0000u : 0x0000ffffu;
    v4u m;
#pragma unroll
    for (int v = 0; v < 4; ++v) m[v] = (e >> 1) == v ? h : 0u;
    return m;
}
template <int I0> __device__ __forceinline__ void gemv8(const v4u (&w)[32], const unsigned (&xa)[32], v4u m, v4f& a0, v4f& a1) {
#pragma unroll
    for (int i = I0; i < I0 + 8; i += 2) {
        v4u p = {xa[i] & m[0], xa[i] & m[1], xa[i] & m[2], xa[i] & m[3]};
        a0 = mfma(p, w[i], a0);
        v4u q = {xa[i + 1] & m[0], xa[i + 1] & m[1], xa[i + 1] & m[2], xa[i + 1] & m[3]};
        a1 = mfma(q, w[i + 1], a1);
    }
}
template <int K> __device__ __forceinline__ void issue8(v4u (&w)[32], rsrc_t r, int off, int ld4rows) {
#pragma unroll
    for (int i = 8 * K; i < 8 * K + 8; ++i) w[i] = ld4<NT>(r, off + i * ld4rows);
    SB();
}

__global__ void __launch_bounds__(512, 2)
fused_decode(const char* __restrict__ xp, const char* __restrict__ kp, const char* __restrict__ vp, const char* __restrict__ wqp,
             const char* __restrict__ wop, const char* __restrict__ w1p, const char* __restrict__ w2p, char* __restrict__ outp, char* ws) {
    __shared__ Lds L;
    const int lane = threadIdx.x & 63, wave = __builtin_amdgcn_readfirstlane(threadIdx.x >> 6);
    const int blk = blockIdx.x, x = blk & 7, j = blk >> 3, kvh = x >> 1, b = x & 1, u = b * 32 + j;
    const int head = kvh * 8 + (u >> 3), k8 = j & 7;
    const int gq = j >> 3;
    const bool heavy = !((x & 1) && gq == 3);
    const int grp = x * 4 + gq - (x >> 1);
    const int g = lane >> 4, n = lane & 15;
    const rsrc_t rw = mk(ws, WS_END);
    unsigned* ctl = L.ctl;
    ST(0);

    if (wave == 0) {
        v4u w[32];
        const rsrc_t r = mk(wqp, 1024 * 8192);
        const int off = (k8 * 128 + g) * 8192 + head * 256 + n * 16;
        issue8<0>(w, r, off, 4 * 8192); issue8<1>(w, r, off, 4 * 8192); issue8<2>(w, r, off, 4 * 8192); issue8<3>(w, r, off, 4 * 8192);
        BAR();
        VMCNT(24 + EARLY); lds_max(ctl + C_ARR, 1, lane); ST(1);
        VMCNT(16 + EARLY); lds_max(ctl + C_ARR, 2, lane);
        lds_wait_ge(ctl + C_X, 1);
        const unsigned T = lds_get(ctl + C_T);
        unsigned xa[32]; load_tab(L.tx, lane, xa);
        const v4u m = diag_mask(lane);
        v4f a0 = {0, 0, 0, 0}, a1 = {0, 0, 0, 0};
        gemv8<0>(w, xa, m, a0, a1);
        VMCNT(8 + EARLY); lds_max(ctl + C_ARR, 3, lane);
        gemv8<8>(w, xa, m, a0, a1);
        VMCNT(EARLY); lds_max(ctl + C_ARR, 4, lane);
        gemv8<16>(w, xa, m, a0, a1);
        gemv8<24>(w, xa, m, a0, a1);
        ST(2);
        st4(rw, WS_QP + (head * 8 + k8) * 1024 + (g >> 1) * 512 + n * 32 + (g & 1) * 16, tag4(a0 + a1, T));
        stb(rw, lane < 8 ? WS_QF + (kvh * 8 + lane) * FL + u : OOB, T);
        SB();
        const int ocol = (j >> 4) * 1024 + x * 128 + (j & 15) * 8 + (lane & 1) * 4 + (g & 1) * 2;
        const unsigned xo = __builtin_amdgcn_raw_buffer_load_b32(mk(xp, 4096), ocol * 2, 0, 0);
        hint_wait<16>(ws + WS_QF + (kvh * 8 + (u >> 3)) * FL, T);
        lds_max(ctl + C_HINT, 15, lane);
        ST(3);
        lds_wait_ge(ctl + C_P, 4);
        hint_wait<16>(ws + WS_OF + head * FL, T);
        lds_max(ctl + C_HINT, 255, lane);
        ST(4);
        lds_wait_ge(ctl + C_D, heavy ? 2 : 1);
        hint_wait<15>(ws + WS_FF + (x * 4 + gq) * FL, T);
        ST(5);
        const int fo = WS_FP + (x * 64 + (lane >> 1)) * 1024 + j * 32 + (lane & 1) * 16;
        v4f s4, f4;
#pragma unroll 1
        for (int it = 0; it < SPIN; ++it) {
            CB();
            v4u ds = ld4<SC1>(rw, fo), df = ld4<SC1>(rw, lane < 56 ? fo + 32 * 1024 : OOB);
            unsigned bad = 0, bad2 = 0;
            s4 = untag4(ds, T, bad); f4 = untag4(df, T, bad2);
            if (lane >= 56) { bad2 = 0; f4 = v4f{0, 0, 0, 0}; }
            if (all_ok(bad | bad2)) break;
        }
        ST(6);
        if (blk == 0) st1(rw, lane == 0 ? WS_EP : OOB, T);
        float v0 = swap32_add(s4[0], f4[0]), v1 = swap32_add(s4[1], f4[1]), v2 = swap32_add(s4[2], f4[2]), v3 = swap32_add(s4[3], f4[3]);
        float p0 = swap16_add(v0, v2), p1 = swap16_add(v1, v3);
        p0 += dpp<ROR8>(p0); p1 += dpp<ROR8>(p1);
        p0 += dpp<ROR4>(p0); p1 += dpp<ROR4>(p1);
        p0 += dpp<ROR2>(p0); p1 += dpp<ROR2>(p1);
        auto e0 = __builtin_amdgcn_permlane32_swap(f2u(p0), f2u(p0), false, false);
        auto e1 = __builtin_amdgcn_permlane32_swap(f2u(p1), f2u(p1), false, false);
        float sa = rbf(u2f(e0[0])), fa = rbf(u2f(e0[1])), sb = rbf(u2f(e1[0])), fb = rbf(u2f(e1[1]));
        float ga = rbf(fa / (1.f + __expf(-fa))), gb = rbf(fb / (1.f + __expf(-fb)));
        unsigned o = pk_bf(blo(xo) + rbf(sa * ga), bhi(xo) + rbf(sb * gb));
        __builtin_amdgcn_raw_buffer_store_b32(o, mk(outp, 4096), (lane & 0x2e) == 0 ? ocol * 2 : OOB, 0, 0);
        ST(7);
    } else if (wave == 1) {
        if (lane < 16) ctl[lane] = 0;
        asm volatile("s_waitcnt lgkmcnt(0)" ::: "memory");
        BAR();
        if (heavy) {
            v4u w[32];
            const rsrc_t r = mk(w1p, 1024 * 7168);
            const int off = (k8 * 128 + g) * 7168 + grp * 256 + n * 16;
            lds_wait_ge(ctl + C_X, 1);
            const unsigned T = lds_get(ctl + C_T);
            unsigned xa[32]; load_tab(L.tx, lane, xa);
            const v4u m = diag_mask(lane);
            lds_wait_ge(ctl + C_ARR, 9); ST(1); issue8<0>(w, r, off, 4 * 7168);
            lds_wait_ge(ctl + C_ARR, 10); issue8<1>(w, r, off, 4 * 7168);
            lds_wait_ge(ctl + C_ARR, 11); issue8<2>(w, r, off, 4 * 7168);
            lds_wait_ge(ctl + C_ARR, 12); issue8<3>(w, r, off, 4 * 7168);
            v4f a0 = {0, 0, 0, 0}, a1 = {0, 0, 0, 0};
            VMCNT(24 + EARLY); lds_max(ctl + C_ARR, 13, lane);
            VMCNT(16 + EARLY); lds_max(ctl + C_ARR, 14, lane);
            gemv8<0>(w, xa, m, a0, a1);
            VMCNT(8 + EARLY); lds_max(ctl + C_ARR, 15, lane);
            gemv8<8>(w, xa, m, a0, a1);
            VMCNT(EARLY); lds_max(ctl + C_ARR, 16, lane);
            gemv8<16>(w, xa, m, a0, a1);
            gemv8<24>(w, xa, m, a0, a1);
            ST(2);
            st4(rw, WS_HP + (grp * 8 + k8) * 1024 + (g >> 1) * 512 + n * 32 + (g & 1) * 16, tag4(a0 + a1, T));
            stb(rw, lane == 0 ? WS_HF + grp * FL + k8 : OOB, T);
            SB();
            hint_wait<2>(ws + WS_HF + grp * FL, T);
            ST(3);
            v4f h;
            const int ho = WS_HP + grp * 8192 + lane * 16;
#pragma unroll 1
            for (int it = 0; it < SPIN; ++it) {
                CB();
                v4u d[8];
#pragma unroll
                for (int i = 0; i < 8; ++i) d[i] = ld4<SC1>(rw, ho + i * 1024);
                unsigned bad = 0;
                h = untag4(d[0], T, bad);
#pragma unroll
                for (int i = 1; i < 8; ++i) h += untag4(d[i], T, bad);
                if (all_ok(bad)) break;
            }
#pragma unroll
            for (int t = 0; t < 4; ++t) { float v = fmaxf(h[t], 0.f); L.th[((lane >> 5) * 4 + t) * TS + (lane & 31)] = pk_bf(v, v); }
            lds_max(ctl + C_H, 1, lane);
            ST(4);
        }
    } else if (wave == 2) {
        const rsrc_t rx = mk(xp, 4096);
        v4u xr[4];
#pragma unroll
        for (int t = 0; t < 4; ++t) xr[t] = ld4<0>(rx, (lane >> 5) * 2048 + t * 512 + (lane & 31) * 16);
        v2u xs = __builtin_amdgcn_raw_buffer_load_b64(rx, (lane >> 5) * 2048 + k8 * 256 + (lane & 31) * 8, 0, 0);
        unsigned E = __builtin_amdgcn_raw_buffer_load_b32(rw, WS_EP, 0, SC1);
        SB();
        BAR();
        float xv[32]; float s = 0;
#pragma unroll
        for (int t = 0; t < 4; ++t)
#pragma unroll
            for (int e = 0; e < 4; ++e) { xv[t * 8 + 2 * e] = blo(xr[t][e]); xv[t * 8 + 2 * e + 1] = bhi(xr[t][e]); }
        float sp[4] = {0, 0, 0, 0};
#pragma unroll
        for (int i = 0; i < 32; ++i) sp[i & 3] += xv[i];
        s = (sp[0] + sp[1]) + (sp[2] + sp[3]);
        s += dpp<XOR1>(s); s += dpp<XOR2>(s); s += dpp<HMIR>(s); s += dpp<ROR8>(s); s = swap16_add(s, s);
        const float mean = s * (1.0f / 1024);
        float qp[4] = {0, 0, 0, 0};
#pragma unroll
        for (int i = 0; i < 32; ++i) { float d = xv[i] - mean; qp[i & 3] = fmaf(d, d, qp[i & 3]); }
        float q = (qp[0] + qp[1]) + (qp[2] + qp[3]);
        q += dpp<XOR1>(q); q += dpp<XOR2>(q); q += dpp<HMIR>(q); q += dpp<ROR8>(q); q = swap16_add(q, q);
        const float mb = rbf(mean), den = rbf(sqrtf(rbf(rbf(q * (1.0f / 1024)) + 1e-5f)));
        const float xq[4] = {blo(xs[0]), bhi(xs[0]), blo(xs[1]), bhi(xs[1])};
#pragma unroll
        for (int t = 0; t < 4; ++t) { float v = rbf(xq[t] - mb) / den; L.tx[((lane >> 5) * 4 + t) * TS + (lane & 31)] = pk_bf(v, v); }
        const unsigned T = __builtin_amdgcn_readfirstlane(E) + 0x01010101u;
        if (lane == 0) ctl[C_T] = T;
        lds_max(ctl + C_X, 1, lane);
        ST(1);
        v4u w[32];
        const rsrc_t r = mk(wop, 4096 * 2048);
        const int off = (head * 128 + g) * 2048 + k8 * 256 + n * 16;
        const int g0 = heavy ? 13 : 9;
        lds_wait_ge(ctl + C_ARR, g0); ST(2); issue8<0>(w, r, off, 4 * 2048);
        lds_wait_ge(ctl + C_ARR, g0 + 1); issue8<1>(w, r, off, 4 * 2048);
        lds_wait_ge(ctl + C_ARR, g0 + 2); issue8<2>(w, r, off, 4 * 2048);
        lds_wait_ge(ctl + C_ARR, g0 + 3); issue8<3>(w, r, off, 4 * 2048);
        VMCNT(24 + EARLY); lds_max(ctl + C_ARR, 17, lane);
        VMCNT(16 + EARLY); lds_max(ctl + C_ARR, 18, lane);
        VMCNT(8 + EARLY); lds_max(ctl + C_ARR, 19, lane);
        VMCNT(EARLY); lds_max(ctl + C_ARR, 20, lane);
        ST(3);
        const v4u m = diag_mask(lane);
        lds_wait_ge(ctl + C_O, 4);
        ST(4);
        unsigned xa[32]; load_tab(L.to, lane, xa);
        v4f a0 = {0, 0, 0, 0}, a1 = {0, 0, 0, 0};
        gemv8<0>(w, xa, m, a0, a1); gemv8<8>(w, xa, m, a0, a1); gemv8<16>(w, xa, m, a0, a1); gemv8<24>(w, xa, m, a0, a1);
        st4(rw, WS_FP + (k8 * 64 + head) * 1024 + (g >> 1) * 512 + n * 32 + (g & 1) * 16, tag4(a0 + a1, T));
        stb(rw, lane < 4 ? WS_FF + (k8 * 4 + lane) * FL + head : OOB, T);
        lds_add(ctl + C_D, 1, lane);
        ST(5);
    } else if (wave == 3) {
        BAR();
        if (heavy) {
            v4u w[32];
            const rsrc_t r = mk(w2p, 3584 * 2048);
            const int off = (grp * 128 + g) * 2048 + k8 * 256 + n * 16;
            lds_wait_ge(ctl + C_ARR, 17); ST(1); issue8<0>(w, r, off, 4 * 2048);
            lds_wait_ge(ctl + C_ARR, 18); issue8<1>(w, r, off, 4 * 2048);
            lds_wait_ge(ctl + C_ARR, 19); issue8<2>(w, r, off, 4 * 2048);
            lds_wait_ge(ctl + C_ARR, 20); issue8<3>(w, r, off, 4 * 2048);
            const v4u m = diag_mask(lane);
            lds_wait_ge(ctl + C_H, 1);
            ST(2);
            const unsigned T = lds_get(ctl + C_T);
            unsigned xa[32]; load_tab(L.th, lane, xa);
            v4f a0 = {0, 0, 0, 0}, a1 = {0, 0, 0, 0};
            gemv8<0>(w, xa, m, a0, a1); gemv8<8>(w, xa, m, a0, a1); gemv8<16>(w, xa, m, a0, a1); gemv8<24>(w, xa, m, a0, a1);
            ST(3);
            st4(rw, WS_FP + (k8 * 64 + 32 + grp) * 1024 + (g >> 1) * 512 + n * 32 + (g & 1) * 16, tag4(a0 + a1, T));
            stb(rw, lane < 4 ? WS_FF + (k8 * 4 + lane) * FL + 32 + grp : OOB, T);
            lds_add(ctl + C_D, 1, lane);
        }
    } else {
        const int a = wave - 4;
        const size_t kvo = ((size_t)(b * 4 + kvh) * SK + (SK - WIN_KEYS) + j * 128 + a * 32) * 256;
        const rsrc_t rk = mk(kp + kvo, 8192), rv = mk(vp + kvo, 8192);
        v4u kr[8], vr[8];
        BAR();
        lds_wait_ge(ctl + C_ARR, a + 1);
        ST(1);
#pragma unroll
        for (int i = 0; i < 8; ++i) kr[i] = ld4<NT>(rk, ((i >> 1) * 8 + (n >> 1)) * 256 + (i & 1) * 128 + (n & 1) * 64 + g * 16);
        VMCNT(EARLY);
        ST(2);
#pragma unroll
        for (int e = 0; e < 8; ++e) vr[e] = ld4<NT>(rv, (8 * (e >> 1) + 2 * g + (e & 1)) * 256 + n * 16);
        SB();
        bool vs = false;
#pragma unroll 1
        for (int it = 0; it < SPIN; ++it) {
            if (lds_get(ctl + C_HINT) >= 15) break;
            if (!vs && vm_low() <= EARLY) { vs = true; lds_max(ctl + C_ARR, 9 + a, lane); }
            __builtin_amdgcn_s_sleep(1);
        }
        CB();
        ST(3);
        const unsigned T = lds_get(ctl + C_T);
        {
            const int qo = WS_QP + (kvh * 8 + 2 * a + (lane >> 5)) * 8192 + b * 512 + (lane & 31) * 16;
            v4u d[8];
#pragma unroll
            for (int i = 0; i < 8; ++i) d[i] = ld4<SC1>(rw, qo + i * 1024);
            VMCNT(8 + EARLY);
            if (!vs) lds_max(ctl + C_ARR, 9 + a, lane);
            v4f q; unsigned bad = 0;
            q = untag4(d[0], T, bad);
#pragma unroll
            for (int i = 1; i < 8; ++i) q += untag4(d[i], T, bad);
#pragma unroll 1
            for (int it = 0; it < SPIN && !all_ok(bad); ++it) {
                CB();
#pragma unroll
                for (int i = 0; i < 8; ++i) d[i] = ld4<SC1>(rw, qo + i * 1024);
                bad = 0;
                q = untag4(d[0], T, bad);
#pragma unroll
                for (int i = 1; i < 8; ++i) q += untag4(d[i], T, bad);
            }
            *(v2u*)(L.qt + a * 128 + lane * 2) = v2u{pk_bf(q[0], q[1]), pk_bf(q[2], q[3])};
            lds_add(ctl + C_Q, 1, lane);
        }
        ST(4);
        v4u vt[8];
#pragma unroll
        for (int d = 0; d < 8; ++d)
#pragma unroll
            for (int e = 0; e < 4; ++e)
                vt[d][e] = __builtin_amdgcn_perm(vr[2 * e + 1][d >> 1], vr[2 * e][d >> 1], (d & 1) ? 0x07060302u : 0x05040100u);
#pragma unroll
        for (int d = 0; d < 8; ++d) asm volatile("" : "+v"(vt[d]));
        lds_wait_ge(ctl + C_Q, 4);
        ST(5);
        const v4u qb0 = *(const v4u*)(L.qt + (n >> 1) * 64 + (n & 1) * 16 + g * 4);
        const v4u qb1 = *(const v4u*)(L.qt + (n >> 1) * 64 + 32 + (n & 1) * 16 + g * 4);
        const bool odd = n & 1;
        float sc[4];
#pragma unroll
        for (int kg = 0; kg < 4; ++kg) {
            v4f d = mfma(kr[2 * kg], qb0, v4f{0, 0, 0, 0});
            d = mfma(kr[2 * kg + 1], qb1, d);
            float snd = odd ? d[1] : d[2], keep = odd ? d[3] : d[0];
            sc[kg] = rbf(rbf(keep + dpp<XOR1>(snd)) * 0.08838834764831845f);
        }
        float mx = fmaxf(fmaxf(sc[0], sc[1]), fmaxf(sc[2], sc[3]));
        mx = fmaxf(mx, dpp<XOR1>(mx)); mx = swap16_max(mx); mx = swap32_max(mx);
        float pe[4]; v4u pa;
        const unsigned pm = odd ? 0xffff0000u : 0x0000ffffu;
#pragma unroll
        for (int kg = 0; kg < 4; ++kg) { pe[kg] = __expf(sc[kg] - mx); pa[kg] = pk_bf(pe[kg], pe[kg]) & pm; }
        float ls = (pe[0] + pe[1]) + (pe[2] + pe[3]);
        ls += dpp<XOR1>(ls); ls = swap16_add(ls, ls); ls = swap32_add(ls, ls);
        v4f oa[2], ob[2];
#pragma unroll
        for (int d = 0; d < 8; ++d) {
            v4f r = mfma(pa, vt[d], v4f{0, 0, 0, 0});
            oa[d >> 2][d & 3] = r[0] + r[1]; ob[d >> 2][d & 3] = r[2] + r[3];
        }
        float* ow = L.ow[a] + g * 256 + n * 4;
        *(v4f*)(ow) = oa[0]; *(v4f*)(ow + 64) = oa[1]; *(v4f*)(ow + 128) = ob[0]; *(v4f*)(ow + 192) = ob[1];
        if (lane < 16 && !odd) { L.ml[n >> 1][a][0] = mx; L.ml[n >> 1][a][1] = ls; }
        lds_add(ctl + C_M, 1, lane);
        ST(6);
        lds_wait_ge(ctl + C_M, 4);
        {
            const int hh = 2 * a + (lane >> 5);
            const v4f m0 = *(const v4f*)&L.ml[hh][0][0], m1 = *(const v4f*)&L.ml[hh][2][0];
            v4f o4[4];
#pragma unroll
            for (int w = 0; w < 4; ++w) o4[w] = *(const v4f*)(L.ow[w] + a * 256 + lane * 4);
            const float M = fmaxf(fmaxf(m0[0], m0[2]), fmaxf(m1[0], m1[2]));
            const float c0 = __expf(m0[0] - M), c1 = __expf(m0[2] - M), c2 = __expf(m1[0] - M), c3 = __expf(m1[2] - M);
            const float Ls = (m0[1] * c0 + m0[3] * c1) + (m1[1] * c2 + m1[3] * c3);
            const v4f o = (o4[0] * c0 + o4[1] * c1) + (o4[2] * c2 + o4[3] * c3);
            const int rec = ((kvh * 8 + hh) * 2 + b) * 32 + j;
            st4(rw, WS_OP + rec * 512 + n * 32 + (g & 1) * 16, tag4(o, T));
            st4(rw, (lane & 31) == 0 ? WS_ML + rec * 16 : OOB, v4u{f2u(M), f2u(Ls), T, T});
            stb(rw, (lane & 31) == 0 ? WS_OF + (kvh * 8 + hh) * FL + u : OOB, T);
            lds_add(ctl + C_P, 1, lane);
        }
        ST(7);
        lds_wait_ge(ctl + C_HINT, 255);
        ST(8);
        {
            const int rec0 = (head * 2 + (a >> 1)) * 32;
            const int oo = WS_OP + (rec0 + g) * 512 + (a & 1) * 256 + n * 16, mo = WS_ML + (rec0 + (n & 7) * 4 + g) * 16;
            v4u d[8], mv; unsigned bad = 0; v4f v[8];
            mv = ld4<SC1>(rw, mo);
#pragma unroll
            for (int i = 0; i < 8; ++i) d[i] = ld4<SC1>(rw, oo + i * 2048);
#pragma unroll
            for (int i = 0; i < 8; ++i) v[i] = untag4(d[i], T, bad);
            if (mv[2] != T || mv[3] != T) bad = 1;
#pragma unroll 1
            for (int it = 0; it < SPIN && !all_ok(bad); ++it) {
                CB();
                mv = ld4<SC1>(rw, mo);
#pragma unroll
                for (int i = 0; i < 8; ++i) d[i] = ld4<SC1>(rw, oo + i * 2048);
                bad = 0;
#pragma unroll
                for (int i = 0; i < 8; ++i) v[i] = untag4(d[i], T, bad);
                if (mv[2] != T || mv[3] != T) bad = 1;
            }
            ST(9);
            float M = u2f(mv[0]);
            M = fmaxf(M, dpp<XOR1>(M)); M = fmaxf(M, dpp<XOR2>(M)); M = fmaxf(M, dpp<HMIR>(M)); M = swap16_max(M); M = swap32_max(M);
            const float c = __expf(u2f(mv[0]) - M);
            float Ls = u2f(mv[1]) * c;
            Ls += dpp<XOR1>(Ls); Ls += dpp<XOR2>(Ls); Ls += dpp<HMIR>(Ls); Ls = swap16_add(Ls, Ls); Ls = swap32_add(Ls, Ls);
            v4f acc = v[0] * dpp<BCAST + 0>(c);
            acc += v[1] * dpp<BCAST + 1>(c); acc += v[2] * dpp<BCAST + 2>(c); acc += v[3] * dpp<BCAST + 3>(c);
            acc += v[4] * dpp<BCAST + 4>(c); acc += v[5] * dpp<BCAST + 5>(c); acc += v[6] * dpp<BCAST + 6>(c); acc += v[7] * dpp<BCAST + 7>(c);
            float r0 = swap32_add(acc[0], acc[2]), r1 = swap32_add(acc[1], acc[3]);
            float o = swap16_add(r0, r1) / Ls;
            L.to[((a >> 1) * 4 + g) * TS + (a & 1) * 16 + n] = pk_bf(o, o);
            lds_add(ctl + C_O, 1, lane);
        }
        ST(10);
    }
}

extern "C" void kernel_launch(void* const* d_in, const int* in_sizes, int n_in, void* d_out, int out_size, void* d_ws, size_t ws_size,
                              hipStream_t stream) {
    fused_decode<<<256, 512, 0, stream>>>((const char*)d_in[0], (const char*)d_in[1], (const char*)d_in[2], (const char*)d_in[3],
                                          (const char*)d_in[4], (const char*)d_in[6], (const char*)d_in[7], (char*)d_out, (char*)d_ws);
}
```

```cpp
#define LW 0
#define LI 0
#include <hip/hip_runtime.h>
#include <hip/hip_bf16.h>
#include <stdint.h>

typedef unsigned v4u __attribute__((ext_vector_type(4)));
typedef unsigned v2u __attribute__((ext_vector_type(2)));
typedef float v4f __attribute__((ext_vector_type(4)));
typedef __bf16 v8bf __attribute__((ext_vector_type(8)));
typedef unsigned v16u __attribute__((ext_vector_type(16)));
typedef unsigned v8u __attribute__((ext_vector_type(8)));
typedef unsigned long long u64;
typedef __amdgpu_buffer_rsrc_t rsrc_t;

#define HSLEEP 1
#define EARLY 2
#define SB() __builtin_amdgcn_sched_barrier(0)
#define BAR() do { SB(); __builtin_amdgcn_s_barrier(); SB(); } while (0)
#define CB() asm volatile("" ::: "memory")
#define VMCNT(n) do { SB(); asm volatile("s_waitcnt vmcnt(%0)" :: "n"(n) : "memory"); SB(); } while (0)

constexpr int SK = 32768, WIN_KEYS = 4096;
constexpr int NT = 2, SC1 = 16;
constexpr int OOB = 0x40000000;
constexpr int SPIN = 1 << 22;
constexpr int WS_EP = 0, FL = 4352, WS_QF = 0x210000, WS_OF = WS_QF + 32 * FL, WS_HF = WS_OF + 32 * FL, WS_FF = WS_HF + 32 * FL;
constexpr int WS_QP = 0x10000, WS_HP = 0x50000, WS_ML = 0x88000, WS_FP = 0x90000, WS_OP = 0x110000, WS_END = WS_FF + 32 * FL;
constexpr int HSPIN = 200;
enum { C_ARR = 0, C_X = 1, C_HINT = 2, C_Q = 3, C_M = 4, C_O = 5, C_H = 6, C_T = 7, C_P = 8, C_D = 9 };
constexpr int TS = 36;

struct alignas(16) Lds {
    unsigned ctl[16];
    unsigned tx[8 * TS], th[8 * TS], to[8 * TS];
    unsigned qt[512];
    float ml[8][4][2];
    float ow[4][1024];
};

__device__ __forceinline__ rsrc_t mk(const void* p, int n) { return __builtin_amdgcn_make_buffer_rsrc((void*)p, 0, n, 0x00020000); }
template <int AUX> __device__ __forceinline__ v4u ld4(rsrc_t r, int off) { return __builtin_amdgcn_raw_buffer_load_b128(r, off, 0, AUX); }
__device__ __forceinline__ void st4(rsrc_t r, int off, v4u v) { __builtin_amdgcn_raw_buffer_store_b128(v, r, off, 0, SC1); }
__device__ __forceinline__ void st1(rsrc_t r, int off, unsigned v) { __builtin_amdgcn_raw_buffer_store_b32(v, r, off, 0, SC1); }
__device__ __forceinline__ float u2f(unsigned u) { return __uint_as_float(u); }
__device__ __forceinline__ unsigned f2u(float f) { return __float_as_uint(f); }
__device__ __forceinline__ unsigned pk_bf(float lo, float hi) { unsigned r; asm("v_cvt_pk_bf16_f32 %0, %1, %2" : "=v"(r) : "v"(lo), "v"(hi)); return r; }
__device__ __forceinline__ float rbf(float f) { return u2f(pk_bf(f, f) & 0xffff0000u); }
__device__ __forceinline__ float blo(unsigned u) { return u2f(u << 16); }
__device__ __forceinline__ float bhi(unsigned u) { return u2f(u & 0xffff0000u); }
template <int CTRL> __device__ __forceinline__ float dpp(float v) { return u2f(__builtin_amdgcn_update_dpp(0u, f2u(v), CTRL, 0xf, 0xf, true)); }
__device__ __forceinline__ v4f mfma(v4u a, v4u b, v4f c) { return __builtin_amdgcn_mfma_f32_16x16x32_bf16(__builtin_bit_cast(v8bf, a), __builtin_bit_cast(v8bf, b), c, 0, 0, 0); }
__device__ __forceinline__ float swap32_add(float a, float b) { auto r = __builtin_amdgcn_permlane32_swap(f2u(a), f2u(b), false, false); return u2f(r[0]) + u2f(r[1]); }
__device__ __forceinline__ float swap16_add(float a, float b) { auto r = __builtin_amdgcn_permlane16_swap(f2u(a), f2u(b), false, false); return u2f(r[0]) + u2f(r[1]); }
__device__ __forceinline__ float swap32_max(float a) { auto r = __builtin_amdgcn_permlane32_swap(f2u(a), f2u(a), false, false); return fmaxf(u2f(r[0]), u2f(r[1])); }
__device__ __forceinline__ float swap16_max(float a) { auto r = __builtin_amdgcn_permlane16_swap(f2u(a), f2u(a), false, false); return fmaxf(u2f(r[0]), u2f(r[1])); }
constexpr int XOR1 = 0xB1, XOR2 = 0x4E, HMIR = 0x141, ROR8 = 0x128, ROR4 = 0x124, ROR2 = 0x122, BCAST = 0x150;

__device__ __forceinline__ unsigned lds_get(unsigned* p) { return __builtin_amdgcn_readfirstlane(__hip_atomic_load(p, __ATOMIC_RELAXED, __HIP_MEMORY_SCOPE_WORKGROUP)); }
__device__ __forceinline__ void lds_wait_ge(unsigned* p, unsigned v) {
    CB();
#pragma unroll 1
    for (int it = 0; it < SPIN; ++it) { if (lds_get(p) >= v) break; __builtin_amdgcn_s_sleep(1); }
    CB();
}
__device__ __forceinline__ void lds_max(unsigned* p, unsigned v, int lane) { CB(); if (lane == 0) __hip_atomic_fetch_max(p, v, __ATOMIC_RELAXED, __HIP_MEMORY_SCOPE_WORKGROUP); CB(); }
__device__ __forceinline__ void lds_add(unsigned* p, unsigned v, int lane) { CB(); if (lane == 0) __hip_atomic_fetch_add(p, v, __ATOMIC_RELAXED, __HIP_MEMORY_SCOPE_WORKGROUP); CB(); }
__device__ __forceinline__ unsigned vm_low() { return __builtin_amdgcn_s_getreg(7 | (0 << 6) | (3 << 11)); }

template <int NW> __device__ __forceinline__ void hint_wait(const char* a, unsigned T) {
    const unsigned t4 = (T & 0xffu) * 0x01010101u;
#pragma unroll 1
    for (int it = 0; it < HSPIN; ++it) {
        v16u f; asm volatile("s_load_dwordx16 %0, %1, 0x0 glc\n\ts_waitcnt lgkmcnt(0)" : "=s"(f) : "s"(a) : "memory");
        unsigned d = 0;
#pragma unroll
        for (int i = 0; i < NW; ++i) d |= f[i] ^ t4;
        if (d == 0) break;
        __builtin_amdgcn_s_sleep(HSLEEP);
    }
}
__device__ __forceinline__ void stb(rsrc_t r, int off, unsigned v) { __builtin_amdgcn_raw_buffer_store_b8((unsigned char)v, r, off, 0, SC1); }
__device__ __forceinline__ v4u tag4(v4f v, unsigned T) {
    v4u r;
#pragma unroll
    for (int i = 0; i < 4; ++i) r[i] = (f2u(v[i]) & 0xffffff00u) | ((T >> (8 * i)) & 0xffu);
    return r;
}
__device__ __forceinline__ v4f untag4(v4u d, unsigned T, unsigned& bad) {
    v4f r;
#pragma unroll
    for (int i = 0; i < 4; ++i) { unsigned t = d[i] ^ ((T >> (8 * i)) & 0xffu); bad |= t; r[i] = u2f(t); }
    return r;
}
__device__ __forceinline__ bool all_ok(unsigned bad) { return __builtin_amdgcn_ballot_w64((bad & 0xffu) != 0) == 0; }

__device__ __forceinline__ u64 now() { u64 t; asm volatile("s_memrealtime %0\n\ts_waitcnt lgkmcnt(0)" : "=s"(t) :: "memory"); return t; }
#define ST(i) do { if ((i) == LI && (wave >= 4 ? 4 : wave) == LW) { SB(); tleak = now(); SB(); } } while (0)

__device__ __forceinline__ void load_tab(const unsigned* tab, int lane, unsigned (&xa)[32]) {
    const v4u* t = (const v4u*)(tab + (((lane >> 3) & 1) * 4 + (lane >> 4)) * TS);
#pragma unroll
    for (int v = 0; v < 8; ++v) { v4u q = t[v]; xa[4 * v] = q[0]; xa[4 * v + 1] = q[1]; xa[4 * v + 2] = q[2]; xa[4 * v + 3] = q[3]; }
}
__device__ __forceinline__ v4u diag_mask(int lane) {
    int e = lane & 7; unsigned h = (e & 1) ? 0xffff0000u : 0x0000ffffu;
    v4u m;
#pragma unroll
    for (int v = 0; v < 4; ++v) m[v] = (e >> 1) == v ? h : 0u;
    return m;
}
template <int I0> __device__ __forceinline__ void gemv8(const v4u (&w)[32], const unsigned (&xa)[32], v4u m, v4f& a0, v4f& a1) {
#pragma unroll
    for (int i = I0; i < I0 + 8; i += 2) {
        v4u p = {xa[i] & m[0], xa[i] & m[1], xa[i] & m[2], xa[i] & m[3]};
        a0 = mfma(p, w[i], a0);
        v4u q = {xa[i + 1] & m[0], xa[i + 1] & m[1], xa[i + 1] & m[2], xa[i + 1] & m[3]};
        a1 = mfma(q, w[i + 1], a1);
    }
}
template <int K> __device__ __forceinline__ void issue8(v4u (&w)[32], rsrc_t r, int off, int ld4rows) {
#pragma unroll
    for (int i = 8 * K; i < 8 * K + 8; ++i) w[i] = ld4<NT>(r, off + i * ld4rows);
    SB();
}

__global__ void __launch_bounds__(512, 2)
fused_decode(const char* __restrict__ xp, const char* __restrict__ kp, const char* __restrict__ vp, const char* __restrict__ wqp,
             const char* __restrict__ wop, const char* __restrict__ w1p, const char* __restrict__ w2p, char* __restrict__ outp, char* ws) {
    __shared__ Lds L;
    const int lane = threadIdx.x & 63, wave = __builtin_amdgcn_readfirstlane(threadIdx.x >> 6);
    const int blk = blockIdx.x, x = blk & 7, j = blk >> 3, kvh = x >> 1, b = x & 1, u = b * 32 + j;
    const int head = kvh * 8 + (u >> 3), k8 = j & 7;
    const int gq = j >> 3;
    const bool heavy = !((x & 1) && gq == 3);
    const int grp = x * 4 + gq - (x >> 1);
    const int g = lane >> 4, n = lane & 15;
    const rsrc_t rw = mk(ws, WS_END);
    unsigned* ctl = L.ctl;
    u64 tleak = 0;
    ST(0);

    if (wave == 0) {
        v4u w[32];
        const rsrc_t r = mk(wqp, 1024 * 8192);
        const int off = (k8 * 128 + g) * 8192 + head * 256 + n * 16;
        issue8<0>(w, r, off, 4 * 8192); issue8<1>(w, r, off, 4 * 8192); issue8<2>(w, r, off, 4 * 8192); issue8<3>(w, r, off, 4 * 8192);
        BAR();
        VMCNT(24 + EARLY); lds_max(ctl + C_ARR, 1, lane); ST(1);
        VMCNT(16 + EARLY); lds_max(ctl + C_ARR, 2, lane);
        lds_wait_ge(ctl + C_X, 1);
        const unsigned T = lds_get(ctl + C_T);
        unsigned xa[32]; load_tab(L.tx, lane, xa);
        const v4u m = diag_mask(lane);
        v4f a0 = {0, 0, 0, 0}, a1 = {0, 0, 0, 0};
        gemv8<0>(w, xa, m, a0, a1);
        VMCNT(8 + EARLY); lds_max(ctl + C_ARR, 3, lane);
        gemv8<8>(w, xa, m, a0, a1);
        VMCNT(EARLY); lds_max(ctl + C_ARR, 4, lane);
        gemv8<16>(w, xa, m, a0, a1);
        gemv8<24>(w, xa, m, a0, a1);
        ST(2);
        st4(rw, WS_QP + (head * 8 + k8) * 1024 + (g >> 1) * 512 + n * 32 + (g & 1) * 16, tag4(a0 + a1, T));
        stb(rw, lane < 8 ? WS_QF + (kvh * 8 + lane) * FL + u : OOB, T);
        SB();
        const int ocol = (j >> 4) * 1024 + x * 128 + (j & 15) * 8 + (lane & 1) * 4 + (g & 1) * 2;
        const unsigned xo = __builtin_amdgcn_raw_buffer_load_b32(mk(xp, 4096), ocol * 2, 0, 0);
        hint_wait<16>(ws + WS_QF + (kvh * 8 + (u >> 3)) * FL, T);
        lds_max(ctl + C_HINT, 15, lane);
        ST(3);
        lds_wait_ge(ctl + C_P, 4);
        hint_wait<16>(ws + WS_OF + head * FL, T);
        lds_max(ctl + C_HINT, 255, lane);
        ST(4);
        lds_wait_ge(ctl + C_D, heavy ? 2 : 1);
        hint_wait<15>(ws + WS_FF + (x * 4 + gq) * FL, T);
        ST(5);
        const int fo = WS_FP + (x * 64 + (lane >> 1)) * 1024 + j * 32 + (lane & 1) * 16;
        v4f s4, f4;
#pragma unroll 1
        for (int it = 0; it < SPIN; ++it) {
            CB();
            v4u ds = ld4<SC1>(rw, fo), df = ld4<SC1>(rw, lane < 56 ? fo + 32 * 1024 : OOB);
            unsigned bad = 0, bad2 = 0;
            s4 = untag4(ds, T, bad); f4 = untag4(df, T, bad2);
            if (lane >= 56) { bad2 = 0; f4 = v4f{0, 0, 0, 0}; }
            if (all_ok(bad | bad2)) break;
        }
        ST(6);
        if (blk == 0) st1(rw, lane == 0 ? WS_EP : OOB, T);
        float v0 = swap32_add(s4[0], f4[0]), v1 = swap32_add(s4[1], f4[1]), v2 = swap32_add(s4[2], f4[2]), v3 = swap32_add(s4[3], f4[3]);
        float p0 = swap16_add(v0, v2), p1 = swap16_add(v1, v3);
        p0 += dpp<ROR8>(p0); p1 += dpp<ROR8>(p1);
        p0 += dpp<ROR4>(p0); p1 += dpp<ROR4>(p1);
        p0 += dpp<ROR2>(p0); p1 += dpp<ROR2>(p1);
        auto e0 = __builtin_amdgcn_permlane32_swap(f2u(p0), f2u(p0), false, false);
        auto e1 = __builtin_amdgcn_permlane32_swap(f2u(p1), f2u(p1), false, false);
        float sa = rbf(u2f(e0[0])), fa = rbf(u2f(e0[1])), sb = rbf(u2f(e1[0])), fb = rbf(u2f(e1[1]));
        float ga = rbf(fa / (1.f + __expf(-fa))), gb = rbf(fb / (1.f + __expf(-fb)));
        unsigned o = pk_bf(blo(xo) + rbf(sa * ga), bhi(xo) + rbf(sb * gb));
        __builtin_amdgcn_raw_buffer_store_b32(o, mk(outp, 4096), (lane & 0x2e) == 0 ? ocol * 2 : OOB, 0, 0);
        ST(7);
    } else if (wave == 1) {
        if (lane < 16) ctl[lane] = 0;
        asm volatile("s_waitcnt lgkmcnt(0)" ::: "memory");
        BAR();
        if (heavy) {
            v4u w[32];
            const rsrc_t r = mk(w1p, 1024 * 7168);
            const int off = (k8 * 128 + g) * 7168 + grp * 256 + n * 16;
            lds_wait_ge(ctl + C_X, 1);
            const unsigned T = lds_get(ctl + C_T);
            unsigned xa[32]; load_tab(L.tx, lane, xa);
            const v4u m = diag_mask(lane);
            lds_wait_ge(ctl + C_ARR, 9); ST(1); issue8<0>(w, r, off, 4 * 7168);
            lds_wait_ge(ctl + C_ARR, 10); issue8<1>(w, r, off, 4 * 7168);
            lds_wait_ge(ctl + C_ARR, 11); issue8<2>(w, r, off, 4 * 7168);
            lds_wait_ge(ctl + C_ARR, 12); issue8<3>(w, r, off, 4 * 7168);
            v4f a0 = {0, 0, 0, 0}, a1 = {0, 0, 0, 0};
            VMCNT(24 + EARLY); lds_max(ctl + C_ARR, 13, lane);
            VMCNT(16 + EARLY); lds_max(ctl + C_ARR, 14, lane);
            gemv8<0>(w, xa, m, a0, a1);
            VMCNT(8 + EARLY); lds_max(ctl + C_ARR, 15, lane);
            gemv8<8>(w, xa, m, a0, a1);
            VMCNT(EARLY); lds_max(ctl + C_ARR, 16, lane);
            gemv8<16>(w, xa, m, a0, a1);
            gemv8<24>(w, xa, m, a0, a1);
            ST(2);
            st4(rw, WS_HP + (grp * 8 + k8) * 1024 + (g >> 1) * 512 + n * 32 + (g & 1) * 16, tag4(a0 + a1, T));
            stb(rw, lane == 0 ? WS_HF + grp * FL + k8 : OOB, T);
            SB();
            hint_wait<2>(ws + WS_HF + grp * FL, T);
            ST(3);
            v4f h;
            const int ho = WS_HP + grp * 8192 + lane * 16;
#pragma unroll 1
            for (int it = 0; it < SPIN; ++it) {
                CB();
                v4u d[8];
#pragma unroll
                for (int i = 0; i < 8; ++i) d[i] = ld4<SC1>(rw, ho + i * 1024);
                unsigned bad = 0;
                h = untag4(d[0], T, bad);
#pragma unroll
                for (int i = 1; i < 8; ++i) h += untag4(d[i], T, bad);
                if (all_ok(bad)) break;
            }
#pragma unroll
            for (int t = 0; t < 4; ++t) { float v = fmaxf(h[t], 0.f); L.th[((lane >> 5) * 4 + t) * TS + (lane & 31)] = pk_bf(v, v); }
            lds_max(ctl + C_H, 1, lane);
            ST(4);
        }
    } else if (wave == 2) {
        const rsrc_t rx = mk(xp, 4096);
        v4u xr[4];
#pragma unroll
        for (int t = 0; t < 4; ++t) xr[t] = ld4<0>(rx, (lane >> 5) * 2048 + t * 512 + (lane & 31) * 16);
        v2u xs = __builtin_amdgcn_raw_buffer_load_b64(rx, (lane >> 5) * 2048 + k8 * 256 + (lane & 31) * 8, 0, 0);
        unsigned E = __builtin_amdgcn_raw_buffer_load_b32(rw, WS_EP, 0, SC1);
        SB();
        BAR();
        float xv[32]; float s = 0;
#pragma unroll
        for (int t = 0; t < 4; ++t)
#pragma unroll
            for (int e = 0; e < 4; ++e) { xv[t * 8 + 2 * e] = blo(xr[t][e]); xv[t * 8 + 2 * e + 1] = bhi(xr[t][e]); }
        float sp[4] = {0, 0, 0, 0};
#pragma unroll
        for (int i = 0; i < 32; ++i) sp[i & 3] += xv[i];
        s = (sp[0] + sp[1]) + (sp[2] + sp[3]);
        s += dpp<XOR1>(s); s += dpp<XOR2>(s); s += dpp<HMIR>(s); s += dpp<ROR8>(s); s = swap16_add(s, s);
        const float mean = s * (1.0f / 1024);
        float qp[4] = {0, 0, 0, 0};
#pragma unroll
        for (int i = 0; i < 32; ++i) { float d = xv[i] - mean; qp[i & 3] = fmaf(d, d, qp[i & 3]); }
        float q = (qp[0] + qp[1]) + (qp[2] + qp[3]);
        q += dpp<XOR1>(q); q += dpp<XOR2>(q); q += dpp<HMIR>(q); q += dpp<ROR8>(q); q = swap16_add(q, q);
        const float mb = rbf(mean), den = rbf(sqrtf(rbf(rbf(q * (1.0f / 1024)) + 1e-5f)));
        const float xq[4] = {blo(xs[0]), bhi(xs[0]), blo(xs[1]), bhi(xs[1])};
#pragma unroll
        for (int t = 0; t < 4; ++t) { float v = rbf(xq[t] - mb) / den; L.tx[((lane >> 5) * 4 + t) * TS + (lane & 31)] = pk_bf(v, v); }
        const unsigned T = __builtin_amdgcn_readfirstlane(E) + 0x01010101u;
        if (lane == 0) ctl[C_T] = T;
        lds_max(ctl + C_X, 1, lane);
        ST(1);
        v4u w[32];
        const rsrc_t r = mk(wop, 4096 * 2048);
        const int off = (head * 128 + g) * 2048 + k8 * 256 + n * 16;
        const int g0 = heavy ? 13 : 9;
        lds_wait_ge(ctl + C_ARR, g0); ST(2); issue8<0>(w, r, off, 4 * 2048);
        lds_wait_ge(ctl + C_ARR, g0 + 1); issue8<1>(w, r, off, 4 * 2048);
        lds_wait_ge(ctl + C_ARR, g0 + 2); issue8<2>(w, r, off, 4 * 2048);
        lds_wait_ge(ctl + C_ARR, g0 + 3); issue8<3>(w, r, off, 4 * 2048);
        VMCNT(24 + EARLY); lds_max(ctl + C_ARR, 17, lane);
        VMCNT(16 + EARLY); lds_max(ctl + C_ARR, 18, lane);
        VMCNT(8 + EARLY); lds_max(ctl + C_ARR, 19, lane);
        VMCNT(EARLY); lds_max(ctl + C_ARR, 20, lane);
        ST(3);
        const v4u m = diag_mask(lane);
        lds_wait_ge(ctl + C_O, 4);
        ST(4);
        unsigned xa[32]; load_tab(L.to, lane, xa);
        v4f a0 = {0, 0, 0, 0}, a1 = {0, 0, 0, 0};
        gemv8<0>(w, xa, m, a0, a1); gemv8<8>(w, xa, m, a0, a1); gemv8<16>(w, xa, m, a0, a1); gemv8<24>(w, xa, m, a0, a1);
        st4(rw, WS_FP + (k8 * 64 + head) * 1024 + (g >> 1) * 512 + n * 32 + (g & 1) * 16, tag4(a0 + a1, T));
        stb(rw, lane < 4 ? WS_FF + (k8 * 4 + lane) * FL + head : OOB, T);
        lds_add(ctl + C_D, 1, lane);
        ST(5);
    } else if (wave == 3) {
        BAR();
        if (heavy) {
            v4u w[32];
            const rsrc_t r = mk(w2p, 3584 * 2048);
            const int off = (grp * 128 + g) * 2048 + k8 * 256 + n * 16;
            lds_wait_ge(ctl + C_ARR, 17); ST(1); issue8<0>(w, r, off, 4 * 2048);
            lds_wait_ge(ctl + C_ARR, 18); issue8<1>(w, r, off, 4 * 2048);
            lds_wait_ge(ctl + C_ARR, 19); issue8<2>(w, r, off, 4 * 2048);
            lds_wait_ge(ctl + C_ARR, 20); issue8<3>(w, r, off, 4 * 2048);
            const v4u m = diag_mask(lane);
            lds_wait_ge(ctl + C_H, 1);
            ST(2);
            const unsigned T = lds_get(ctl + C_T);
            unsigned xa[32]; load_tab(L.th, lane, xa);
            v4f a0 = {0, 0, 0, 0}, a1 = {0, 0, 0, 0};
            gemv8<0>(w, xa, m, a0, a1); gemv8<8>(w, xa, m, a0, a1); gemv8<16>(w, xa, m, a0, a1); gemv8<24>(w, xa, m, a0, a1);
            ST(3);
            st4(rw, WS_FP + (k8 * 64 + 32 + grp) * 1024 + (g >> 1) * 512 + n * 32 + (g & 1) * 16, tag4(a0 + a1, T));
            stb(rw, lane < 4 ? WS_FF + (k8 * 4 + lane) * FL + 32 + grp : OOB, T);
            lds_add(ctl + C_D, 1, lane);
        }
    } else {
        const int a = wave - 4;
        const size_t kvo = ((size_t)(b * 4 + kvh) * SK + (SK - WIN_KEYS) + j * 128 + a * 32) * 256;
        const rsrc_t rk = mk(kp + kvo, 8192), rv = mk(vp + kvo, 8192);
        v4u kr[8], vr[8];
        BAR();
        lds_wait_ge(ctl + C_ARR, a + 1);
        ST(1);
#pragma unroll
        for (int i = 0; i < 8; ++i) kr[i] = ld4<NT>(rk, ((i >> 1) * 8 + (n >> 1)) * 256 + (i & 1) * 128 + (n & 1) * 64 + g * 16);
        VMCNT(EARLY);
        ST(2);
#pragma unroll
        for (int e = 0; e < 8; ++e) vr[e] = ld4<NT>(rv, (8 * (e >> 1) + 2 * g + (e & 1)) * 256 + n * 16);
        SB();
        bool vs = false;
#pragma unroll 1
        for (int it = 0; it < SPIN; ++it) {
            if (lds_get(ctl + C_HINT) >= 15) break;
            if (!vs && vm_low() <= EARLY) { vs = true; lds_max(ctl + C_ARR, 9 + a, lane); }
            __builtin_amdgcn_s_sleep(1);
        }
        CB();
        ST(3);
        const unsigned T = lds_get(ctl + C_T);
        {
            const int qo = WS_QP + (kvh * 8 + 2 * a + (lane >> 5)) * 8192 + b * 512 + (lane & 31) * 16;
            v4u d[8];
#pragma unroll
            for (int i = 0; i < 8; ++i) d[i] = ld4<SC1>(rw, qo + i * 1024);
            VMCNT(8 + EARLY);
            if (!vs) lds_max(ctl + C_ARR, 9 + a, lane);
            v4f q; unsigned bad = 0;
            q = untag4(d[0], T, bad);
#pragma unroll
            for (int i = 1; i < 8; ++i) q += untag4(d[i], T, bad);
#pragma unroll 1
            for (int it = 0; it < SPIN && !all_ok(bad); ++it) {
                CB();
#pragma unroll
                for (int i = 0; i < 8; ++i) d[i] = ld4<SC1>(rw, qo + i * 1024);
                bad = 0;
                q = untag4(d[0], T, bad);
#pragma unroll
                for (int i = 1; i < 8; ++i) q += untag4(d[i], T, bad);
            }
            *(v2u*)(L.qt + a * 128 + lane * 2) = v2u{pk_bf(q[0], q[1]), pk_bf(q[2], q[3])};
            lds_add(ctl + C_Q, 1, lane);
        }
        ST(4);
        v4u vt[8];
#pragma unroll
        for (int d = 0; d < 8; ++d)
#pragma unroll
            for (int e = 0; e < 4; ++e)
                vt[d][e] = __builtin_amdgcn_perm(vr[2 * e + 1][d >> 1], vr[2 * e][d >> 1], (d & 1) ? 0x07060302u : 0x05040100u);
#pragma unroll
        for (int d = 0; d < 8; ++d) asm volatile("" : "+v"(vt[d]));
        lds_wait_ge(ctl + C_Q, 4);
        ST(5);
        const v4u qb0 = *(const v4u*)(L.qt + (n >> 1) * 64 + (n & 1) * 16 + g * 4);
        const v4u qb1 = *(const v4u*)(L.qt + (n >> 1) * 64 + 32 + (n & 1) * 16 + g * 4);
        const bool odd = n & 1;
        float sc[4];
#pragma unroll
        for (int kg = 0; kg < 4; ++kg) {
            v4f d = mfma(kr[2 * kg], qb0, v4f{0, 0, 0, 0});
            d = mfma(kr[2 * kg + 1], qb1, d);
            float snd = odd ? d[1] : d[2], keep = odd ? d[3] : d[0];
            sc[kg] = rbf(rbf(keep + dpp<XOR1>(snd)) * 0.08838834764831845f);
        }
        float mx = fmaxf(fmaxf(sc[0], sc[1]), fmaxf(sc[2], sc[3]));
        mx = fmaxf(mx, dpp<XOR1>(mx)); mx = swap16_max(mx); mx = swap32_max(mx);
        float pe[4]; v4u pa;
        const unsigned pm = odd ? 0xffff0000u : 0x0000ffffu;
#pragma unroll
        for (int kg = 0; kg < 4; ++kg) { pe[kg] = __expf(sc[kg] - mx); pa[kg] = pk_bf(pe[kg], pe[kg]) & pm; }
        float ls = (pe[0] + pe[1]) + (pe[2] + pe[3]);
        ls += dpp<XOR1>(ls); ls = swap16_add(ls, ls); ls = swap32_add(ls, ls);
        v4f oa[2], ob[2];
#pragma unroll
        for (int d = 0; d < 8; ++d) {
            v4f r = mfma(pa, vt[d], v4f{0, 0, 0, 0});
            oa[d >> 2][d & 3] = r[0] + r[1]; ob[d >> 2][d & 3] = r[2] + r[3];
        }
        float* ow = L.ow[a] + g * 256 + n * 4;
        *(v4f*)(ow) = oa[0]; *(v4f*)(ow + 64) = oa[1]; *(v4f*)(ow + 128) = ob[0]; *(v4f*)(ow + 192) = ob[1];
        if (lane < 16 && !odd) { L.ml[n >> 1][a][0] = mx; L.ml[n >> 1][a][1] = ls; }
        lds_add(ctl + C_M, 1, lane);
        ST(6);
        lds_wait_ge(ctl + C_M, 4);
        {
            const int hh = 2 * a + (lane >> 5);
            const v4f m0 = *(const v4f*)&L.ml[hh][0][0], m1 = *(const v4f*)&L.ml[hh][2][0];
            v4f o4[4];
#pragma unroll
            for (int w = 0; w < 4; ++w) o4[w] = *(const v4f*)(L.ow[w] + a * 256 + lane * 4);
            const float M = fmaxf(fmaxf(m0[0], m0[2]), fmaxf(m1[0], m1[2]));
            const float c0 = __expf(m0[0] - M), c1 = __expf(m0[2] - M), c2 = __expf(m1[0] - M), c3 = __expf(m1[2] - M);
            const float Ls = (m0[1] * c0 + m0[3] * c1) + (m1[1] * c2 + m1[3] * c3);
            const v4f o = (o4[0] * c0 + o4[1] * c1) + (o4[2] * c2 + o4[3] * c3);
            const int rec = ((kvh * 8 + hh) * 2 + b) * 32 + j;
            st4(rw, WS_OP + rec * 512 + n * 32 + (g & 1) * 16, tag4(o, T));
            st4(rw, (lane & 31) == 0 ? WS_ML + rec * 16 : OOB, v4u{f2u(M), f2u(Ls), T, T});
            stb(rw, (lane & 31) == 0 ? WS_OF + (kvh * 8 + hh) * FL + u : OOB, T);
            lds_add(ctl + C_P, 1, lane);
        }
        ST(7);
        lds_wait_ge(ctl + C_HINT, 255);
        ST(8);
        {
            const int rec0 = (head * 2 + (a >> 1)) * 32;
            const int oo = WS_OP + (rec0 + g) * 512 + (a & 1) * 256 + n * 16, mo = WS_ML + (rec0 + (n & 7) * 4 + g) * 16;
            v4u d[8], mv; unsigned bad = 0; v4f v[8];
            mv = ld4<SC1>(rw, mo);
#pragma unroll
            for (int i = 0; i < 8; ++i) d[i] = ld4<SC1>(rw, oo + i * 2048);
#pragma unroll
            for (int i = 0; i < 8; ++i) v[i] = untag4(d[i], T, bad);
            if (mv[2] != T || mv[3] != T) bad = 1;
#pragma unroll 1
            for (int it = 0; it < SPIN && !all_ok(bad); ++it) {
                CB();
                mv = ld4<SC1>(rw, mo);
#pragma unroll
                for (int i = 0; i < 8; ++i) d[i] = ld4<SC1>(rw, oo + i * 2048);
                bad = 0;
#pragma unroll
                for (int i = 0; i < 8; ++i) v[i] = untag4(d[i], T, bad);
                if (mv[2] != T || mv[3] != T) bad = 1;
            }
            ST(9);
            float M = u2f(mv[0]);
            M = fmaxf(M, dpp<XOR1>(M)); M = fmaxf(M, dpp<XOR2>(M)); M = fmaxf(M, dpp<HMIR>(M)); M = swap16_max(M); M = swap32_max(M);
            const float c = __expf(u2f(mv[0]) - M);
            float Ls = u2f(mv[1]) * c;
            Ls += dpp<XOR1>(Ls); Ls += dpp<XOR2>(Ls); Ls += dpp<HMIR>(Ls); Ls = swap16_add(Ls, Ls); Ls = swap32_add(Ls, Ls);
            v4f acc = v[0] * dpp<BCAST + 0>(c);
            acc += v[1] * dpp<BCAST + 1>(c); acc += v[2] * dpp<BCAST + 2>(c); acc += v[3] * dpp<BCAST + 3>(c);
            acc += v[4] * dpp<BCAST + 4>(c); acc += v[5] * dpp<BCAST + 5>(c); acc += v[6] * dpp<BCAST + 6>(c); acc += v[7] * dpp<BCAST + 7>(c);
            float r0 = swap32_add(acc[0], acc[2]), r1 = swap32_add(acc[1], acc[3]);
            float o = swap16_add(r0, r1) / Ls;
            L.to[((a >> 1) * 4 + g) * TS + (a & 1) * 16 + n] = pk_bf(o, o);
            lds_add(ctl + C_O, 1, lane);
        }
        ST(10);
    }
    if (tleak) { for (int it = 0; it < 100000; ++it) { if (now() >= tleak + 4000) break; __builtin_amdgcn_s_sleep(1); } }
}

extern "C" void kernel_launch(void* const* d_in, const int* in_sizes, int n_in, void* d_out, int out_size, void* d_ws, size_t ws_size,
                              hipStream_t stream) {
    fused_decode<<<256, 512, 0, stream>>>((const char*)d_in[0], (const char*)d_in[1], (const char*)d_in[2], (const char*)d_in[3],
                                          (const char*)d_in[4], (const char*)d_in[6], (const char*)d_in[7], (char*)d_out, (char*)d_ws);
}
```

```cpp
#define PACE 232
#define PACE2 205
#define TQ 270
#define TO 530
#define TH 590
#define TF 800
#define GAP4 30
#define CODE_BYTES 18316
#include <hip/hip_runtime.h>
#include <hip/hip_bf16.h>
#include <stdint.h>

typedef unsigned v4u __attribute__((ext_vector_type(4)));
typedef unsigned v16u __attribute__((ext_vector_type(16)));
typedef unsigned v2u __attribute__((ext_vector_type(2)));
typedef float v4f __attribute__((ext_vector_type(4)));
typedef float v2f __attribute__((ext_vector_type(2)));
typedef __bf16 v8bf __attribute__((ext_vector_type(8)));
typedef __bf16 v2bf __attribute__((ext_vector_type(2)));
typedef unsigned long long u64;
typedef __amdgpu_buffer_rsrc_t rsrc_t;

#define GO 50
#define GH 45
#define GS 45
#define GAP3 0
#define GAP5 0
#define NOG 0
#define ONEP 0
#define NOST 0
#define SB() __builtin_amdgcn_sched_barrier(0)
#define CB() asm volatile("" ::: "memory")
#define BAR() do { CB(); SB(); __builtin_amdgcn_s_barrier(); SB(); CB(); } while (0)
#define VMCNT(n) do { SB(); asm volatile("s_waitcnt vmcnt(%0)" :: "n"(n) : "memory"); SB(); } while (0)

constexpr int SK = 32768, WIN_KEYS = 4096;
constexpr int NT = 2, SC1 = 16;
constexpr int OOB = 0x40000000;
constexpr int SPIN = 1 << 22;
constexpr int WS_EP = 0, WS_QP = 0x10000, WS_HP = 0x30000, WS_OP = 0x70000, WS_FP = 0x170000, WS_END = 0x230000;
constexpr int FPS = 93 * 128;
enum { C_X = 1, C_Q = 3, C_M = 4, C_O = 5, C_H = 6, C_T = 7, C_F = 8 };
constexpr int TS = 132;
constexpr int TAB_H = 64, TAB_O = 96;
constexpr int OWS = 132;

struct alignas(16) Lds {
    unsigned ctl[16];
    unsigned tab[8 * TS];
    unsigned qt[512];
    float ml[8][4][2];
    float ow[4][8 * OWS];
};

__device__ __forceinline__ rsrc_t mk(const void* p, int n) { return __builtin_amdgcn_make_buffer_rsrc((void*)p, 0, n, 0x00020000); }
template <int AUX> __device__ __forceinline__ v4u ld4(rsrc_t r, int off, int so = 0) { return __builtin_amdgcn_raw_buffer_load_b128(r, off, so, AUX); }
__device__ __forceinline__ void st4(rsrc_t r, int off, v4u v) { if (NOST) { asm volatile("" :: "v"(v)); return; } __builtin_amdgcn_raw_buffer_store_b128(v, r, off, 0, SC1); }
__device__ __forceinline__ void st1(rsrc_t r, int off, unsigned v) { __builtin_amdgcn_raw_buffer_store_b32(v, r, off, 0, SC1); }
__device__ __forceinline__ float u2f(unsigned u) { return __uint_as_float(u); }
__device__ __forceinline__ unsigned f2u(float f) { return __float_as_uint(f); }
__device__ __forceinline__ unsigned pk_bf(float lo, float hi) { return __builtin_bit_cast(unsigned, __builtin_convertvector(v2f{lo, hi}, v2bf)); }
__device__ __forceinline__ float rbf(float f) { return u2f(pk_bf(0.f, f)); }
__device__ __forceinline__ float blo(unsigned u) { return u2f(u << 16); }
__device__ __forceinline__ float bhi(unsigned u) { return u2f(u & 0xffff0000u); }
template <int CTRL> __device__ __forceinline__ unsigned dppu(unsigned v) { return __builtin_amdgcn_update_dpp(0u, v, CTRL, 0xf, 0xf, true); }
template <int CTRL> __device__ __forceinline__ float dpp(float v) { return u2f(dppu<CTRL>(f2u(v))); }
__device__ __forceinline__ v4f mfma(v4u a, v4u b, v4f c) { return __builtin_amdgcn_mfma_f32_16x16x32_bf16(__builtin_bit_cast(v8bf, a), __builtin_bit_cast(v8bf, b), c, 0, 0, 0); }
__device__ __forceinline__ float fmx(float a, float b) { return __builtin_amdgcn_fmed3f(a, b, __builtin_inff()); }
__device__ __forceinline__ float swap32_add(float a, float b) { auto r = __builtin_amdgcn_permlane32_swap(f2u(a), f2u(b), false, false); return u2f(r[0]) + u2f(r[1]); }
__device__ __forceinline__ float swap16_add(float a, float b) { auto r = __builtin_amdgcn_permlane16_swap(f2u(a), f2u(b), false, false); return u2f(r[0]) + u2f(r[1]); }
__device__ __forceinline__ float swap32_max(float a) { auto r = __builtin_amdgcn_permlane32_swap(f2u(a), f2u(a), false, false); return fmx(u2f(r[0]), u2f(r[1])); }
__device__ __forceinline__ float swap16_max(float a) { auto r = __builtin_amdgcn_permlane16_swap(f2u(a), f2u(a), false, false); return fmx(u2f(r[0]), u2f(r[1])); }
constexpr int XOR1 = 0xB1, XOR2 = 0x4E, HMIR = 0x141, ROR8 = 0x128, ROR4 = 0x124, ROR2 = 0x122, BCAST = 0x150;

__device__ __forceinline__ unsigned lds_get(unsigned* p) { return __builtin_amdgcn_readfirstlane(__hip_atomic_load(p, __ATOMIC_RELAXED, __HIP_MEMORY_SCOPE_WORKGROUP)); }
template <bool SLEEP> __device__ __forceinline__ int lds_wait(unsigned addr, int v) {
    unsigned t, c; int s;
    if (SLEEP)
        asm volatile("s_mov_b32 %2, %5\n1:\n\tds_read_b32 %0, %3\n\ts_waitcnt lgkmcnt(0)\n\tv_readfirstlane_b32 %1, %0\n\ts_cmp_ge_i32 %1, %4\n\t"
                     "s_cbranch_scc1 2f\n\ts_sleep 1\n\ts_sub_u32 %2, %2, 1\n\ts_cbranch_scc0 1b\n2:"
                     : "=&v"(t), "=&s"(s), "=&s"(c) : "v"(addr), "s"(v), "n"(SPIN) : "memory", "scc");
    else
        asm volatile("s_mov_b32 %2, %5\n1:\n\tds_read_b32 %0, %3\n\ts_waitcnt lgkmcnt(0)\n\tv_readfirstlane_b32 %1, %0\n\ts_cmp_ge_i32 %1, %4\n\t"
                     "s_cbranch_scc1 2f\n\ts_sub_u32 %2, %2, 1\n\ts_cbranch_scc0 1b\n2:"
                     : "=&v"(t), "=&s"(s), "=&s"(c) : "v"(addr), "s"(v), "n"(SPIN) : "memory", "scc");
    return s;
}
template <int W> __device__ __forceinline__ void lds_add(unsigned v) {
    asm volatile("s_mov_b64 exec, 1\n\tds_add_u32 %0, %1 offset:%2\n\ts_mov_b64 exec, -1" :: "v"(0), "v"(v), "n"(W * 4) : "memory");
}
__device__ __forceinline__ v4u tag4(v4f v, unsigned T) {
    v4u r;
#pragma unroll
    for (int i = 0; i < 4; ++i) r[i] = __builtin_amdgcn_perm(f2u(v[i]), T, 0x07060500u + i);
    return r;
}
__device__ __forceinline__ v4f asf(v4u d) { return __builtin_bit_cast(v4f, d & 0xffffff00u); }
__device__ __forceinline__ bool any(bool p) { return __builtin_amdgcn_ballot_w64(p) != 0; }
__device__ __forceinline__ unsigned tag_of(v4u d) {
    return __builtin_amdgcn_perm(__builtin_amdgcn_perm(d[3], d[2], 0x0400u), __builtin_amdgcn_perm(d[1], d[0], 0x0400u), 0x05040100u);
}
template <int N> __device__ __forceinline__ void anyval(v4u (&d)[N]) {
#pragma unroll
    for (int i = 0; i < N; ++i) asm volatile("" : "=v"(d[i]));
}
template <int A> struct Aux { static constexpr int v = A; };
template <int ID, int N, class F> __device__ __forceinline__ int gather(v4u (&d)[N], unsigned T, int tries, F ld) {
    int it = 0;
    if (NOG >> ID & 1) { anyval(d); return 0; }
    if (ONEP) tries = 0;
    CB();
#pragma unroll
    for (int i = 0; i < N; ++i) d[i] = ld(i, Aux<0>{});
#pragma unroll 1
    for (;; ++it) {
        bool bad[N], more = false;
#pragma unroll
        for (int i = 0; i < N; ++i) { bad[i] = tag_of(d[i]) != T; more |= bad[i]; }
        if (!any(more) || it >= tries) break;
        CB();
#pragma unroll
        for (int i = 0; i < N; ++i) if (bad[i]) d[i] = ld(i, Aux<SC1>{});
    }
    return it;
}
__device__ __forceinline__ v4f sum8(const v4u (&d)[8]) {
    return ((asf(d[0]) + asf(d[1])) + (asf(d[2]) + asf(d[3]))) + ((asf(d[4]) + asf(d[5])) + (asf(d[6]) + asf(d[7])));
}
__device__ __forceinline__ int clk() { int t; asm volatile("s_memrealtime vcc\n\ts_waitcnt lgkmcnt(0)\n\ts_lshl_b32 %0, vcc_lo, 4" : "=s"(t) :: "vcc", "scc", "memory"); return t; }
template <bool SLEEP> __device__ __forceinline__ void until(int t) {
    SB();
    if (SLEEP)
        asm volatile("1:\n\ts_memrealtime vcc\n\ts_waitcnt lgkmcnt(0)\n\ts_lshl_b32 vcc_lo, vcc_lo, 4\n\ts_sub_u32 vcc_lo, %0, vcc_lo\n\t"
                     "s_cmp_le_i32 vcc_lo, 0\n\ts_cbranch_scc1 3f\n"
                     "2:\n\ts_sub_u32 vcc_lo, vcc_lo, 64\n\ts_cbranch_scc1 1b\n\ts_sleep 1\n\ts_branch 2b\n3:" :: "s"(t) : "vcc", "scc", "memory");
    else
        asm volatile("1:\n\ts_memrealtime vcc\n\ts_waitcnt lgkmcnt(0)\n\ts_lshl_b32 vcc_lo, vcc_lo, 4\n\ts_sub_u32 vcc_lo, vcc_lo, %0\n\t"
                     "s_cmp_lt_i32 vcc_lo, 0\n\ts_cbranch_scc1 1b" :: "s"(t) : "vcc", "scc", "memory");
    SB();
}
#define ST(i)
#define STV(i, v) (void)(v)
__device__ __forceinline__ v4u diag_mask(int lane) {
    const int e = lane & 7; const unsigned h = (e & 1) ? 0xffff0000u : 0x0000ffffu;
    return v4u{(e >> 1) == 0 ? h : 0u, (e >> 1) == 1 ? h : 0u, (e >> 1) == 2 ? h : 0u, (e >> 1) == 3 ? h : 0u};
}
__device__ __forceinline__ v4u frag(unsigned xa, v4u m) { return v4u{xa & m[0], xa & m[1], xa & m[2], xa & m[3]}; }
constexpr int slot(int k) { return (k <= 4 ? k * 8 * PACE : 32 * PACE + (k - 4) * 8 * PACE2) + 16 * ((k >= 3) * GAP3 + (k >= 4) * GAP4 + (k >= 5) * GAP5); }
__device__ __forceinline__ int later(int a, int b) { return a - b > 0 ? a : b; }
__device__ __forceinline__ v4f gemv(const v4u (&w)[32], const v4u* tr, v4u dm) {
    v4u xa[8], f[32];
#pragma unroll
    for (int i = 0; i < 8; ++i) xa[i] = tr[i];
    v4f a0 = {0, 0, 0, 0}, a1 = {0, 0, 0, 0};
#pragma unroll
    for (int i = 0; i < 4; ++i) f[i] = frag(xa[0][i], dm);
    SB();
#pragma unroll
    for (int i = 0; i < 32; ++i) {
        if (i & 1) a1 = mfma(f[i], w[i], a1); else a0 = mfma(f[i], w[i], a0);
        if (i < 28) f[i + 4] = frag(xa[(i + 4) >> 2][i & 3], dm);
        SB();
    }
    return a0 + a1;
}
#define SWX 1
#define SWO 1
#define KVC 2

__global__ void __launch_bounds__(512, 2)
fused_decode(const void*, const void*, const void*, const void*, const void*, const void*, const void*, void*, void*) {
    __shared__ Lds L;
    const int lane = threadIdx.x & 63;
    const int wave = __builtin_amdgcn_readfirstlane(threadIdx.x >> 6);
    const int blk = blockIdx.x, x = blk & 7, j = blk >> 3, kvh = x >> 1, b = x & 1;
    v16u ka; u64 kw, t00;
    asm volatile("s_load_dwordx16 %0, %2, 0x0\n\ts_load_dwordx2 %1, %2, 0x40"
                 : "=&s"(ka), "=&s"(kw) : "s"(__builtin_amdgcn_kernarg_segment_ptr()));
#define ST0()
    unsigned pf;
    {
        u64 pc;
        asm volatile("s_getpc_b64 %1\n\tv_cmp_gt_u32 vcc, %3, %2\n\ts_mov_b64 exec, vcc\n\ts_cbranch_execz 1f\n\tglobal_load_dword %0, %2, %1\n1:\n\ts_mov_b64 exec, -1"
                     : "=v"(pf), "=&s"(pc) : "v"((threadIdx.x ^ 256) * 128), "s"(CODE_BYTES) : "vcc");
    }
    unsigned* ctl = L.ctl;
    const int g = lane >> 4, n = lane & 15;
    const int cb = (j & 16) * 2 + ((j ^ b * 16) & 31), xr = SWX == 1 ? kvh * 2 : SWX == 2 ? x : 0;
    int qoff = kvh * 256 * 8192 + cb * 128, qs = xr << 17, qv[8];
#pragma unroll
    for (int i = 0; i < 8; ++i) qv[i] = g * 8192 + n * 8 + (i >> 1) * 32768 + (i & 1) * (128 * 8192);
    asm volatile("s_waitcnt lgkmcnt(0)\n\ts_memrealtime %2" : "+s"(ka), "+s"(kw), "=&s"(t00), "+s"(qoff), "+s"(qs)
                 : "v"(qv[0]), "v"(qv[1]), "v"(qv[2]), "v"(qv[3]), "v"(qv[4]), "v"(qv[5]), "v"(qv[6]), "v"(qv[7]) : "memory");
    auto arg = [&](int i) { return (char*)((u64)ka[2 * i] | (u64)ka[2 * i + 1] << 32); };
    const char *xp = arg(0), *kp = arg(1), *vp = arg(2), *wqp = arg(3), *wop = arg(4), *w1p = arg(5), *w2p = arg(6);
    char *outp = arg(7), *ws = (char*)kw;
#define GET_T0() asm volatile("s_waitcnt lgkmcnt(0)" : "+s"(t00) :: "memory"); const int t0 = (int)t00 << 4
    const int rg = b * 8 + (j >> 2), wr = SWO ? kvh * 2 : 0;
    const bool heavy = j < 28;
    const rsrc_t rw = mk(ws, WS_END);
    const unsigned* trow = L.tab + (((lane >> 3) & 1) * 4 + g) * TS;
    if (__builtin_expect(wave == 1, 1)) {
        const rsrc_t rt = mk(wqp + qoff, 8192 * 256);
        v2u h[64];
#pragma unroll
        for (int i = 0; i < 8; ++i) h[i] = __builtin_amdgcn_raw_buffer_load_b64(rt, qv[i], qs, NT);
        GET_T0();
        ST0(); ST(13);
        BAR();
#pragma unroll
        for (int c = 1; c < 8; ++c) {
            until<0>(t0 + c * PACE);
#pragma unroll
            for (int i = 0; i < 8; ++i) h[8 * c + i] = __builtin_amdgcn_raw_buffer_load_b64(rt, qv[i], qs ^ c << 17, NT);
        }
        ST(4);
        const v4u dm = diag_mask(lane);
        const int so = (g & 1) ? OOB : WS_QP + (cb >> 1) * 4096 + kvh * 1024 + (g >> 1) * 512 + (cb & 1) * 256 + n * 16;
        asm volatile("" :: "v"(dm), "v"(so));
        lds_wait<0>(C_X * 4, 1);
        ST(5);
        const unsigned T = lds_get(ctl + C_T);
        v4u w[32];
#pragma unroll
        for (int i = 0; i < 32; ++i) w[i] = v4u{h[2 * i][0], h[2 * i][1], h[2 * i + 1][0], h[2 * i + 1][1]};
        v4f q = gemv(w, (const v4u*)(trow + (lane & 4) * 8), dm);
#pragma unroll
        for (int e = 0; e < 4; ++e) q[e] = swap16_add(q[e], q[e]);
        st4(rw, so, tag4(q, T));
        ST(6);
    } else if (__builtin_expect(wave < 4, 1)) {
        const int id2 = x * 28 + j, grp = id2 >> 3;
        unsigned T;
        if (wave == 2) {
            const rsrc_t rx = mk(xp, 4096);
            v4u xr4[4];
#pragma unroll
            for (int t = 0; t < 4; ++t) xr4[t] = ld4<0>(rx, (lane >> 5) * 2048 + (lane & 31) * 16, (t ^ kvh) * 512);
            const unsigned ep = __builtin_amdgcn_raw_buffer_load_b32(rw, WS_EP, 0, SC1);
            ST0(); BAR();
            float xv[32];
#pragma unroll
            for (int t = 0; t < 4; ++t)
#pragma unroll
                for (int e = 0; e < 4; ++e) { xv[t * 8 + 2 * e] = blo(xr4[t][e]); xv[t * 8 + 2 * e + 1] = bhi(xr4[t][e]); }
            v2f sp[4];
#pragma unroll
            for (int t = 0; t < 4; ++t) sp[t] = (v2f{xv[8 * t], xv[8 * t + 1]} + v2f{xv[8 * t + 2], xv[8 * t + 3]}) + (v2f{xv[8 * t + 4], xv[8 * t + 5]} + v2f{xv[8 * t + 6], xv[8 * t + 7]});
            const v2f s2 = (sp[0] + sp[1]) + (sp[2] + sp[3]);
            float s = s2[0] + s2[1];
            s += dpp<XOR1>(s); s += dpp<XOR2>(s); s += dpp<HMIR>(s); s += dpp<ROR8>(s); s = swap16_add(s, s);
            const float mean = s * (1.0f / 1024);
#pragma unroll
            for (int t = 0; t < 4; ++t) {
                sp[t] = v2f{0, 0};
#pragma unroll
                for (int i = 4 * t; i < 4 * t + 4; ++i) { const v2f d = v2f{xv[2 * i], xv[2 * i + 1]} - mean; sp[t] = __builtin_elementwise_fma(d, d, sp[t]); }
            }
            const v2f q2 = (sp[0] + sp[1]) + (sp[2] + sp[3]);
            float q = q2[0] + q2[1];
            q += dpp<XOR1>(q); q += dpp<XOR2>(q); q += dpp<HMIR>(q); q += dpp<ROR8>(q); q = swap16_add(q, q);
            const float mb = rbf(mean), den = rbf(sqrtf(rbf(rbf(q * (1.0f / 1024)) + 1e-5f)));
            unsigned* t = L.tab + (lane >> 5) * 4 * TS + ((lane & 31) * 2 ^ xr * 4);
#pragma unroll
            for (int e = 0; e < 4; ++e) {
                const float v = rbf(xv[e] - mb) / den, w = rbf(xv[e + 4] - mb) / den;
                *(v2u*)(t + e * TS) = v2u{pk_bf(v, v), pk_bf(w, w)};
            }
            T = __builtin_amdgcn_readfirstlane(ep) + 0x01010101u;
            if (lane == 0) ctl[C_T] = T;
            lds_add<C_X>(1);
            ST(1);
        } else {
            if (lane < 16) ctl[lane] = 0;
            ST0();
            asm volatile("s_waitcnt lgkmcnt(0)" ::: "memory");
            BAR();
        }
        GET_T0();
        if (wave != 2) {
            const int h0 = (wave + 1) & 4;
            const int qo = WS_QP + (kvh * 8 + h0) * 4096 + (lane >> 5) * 1024 + b * 512 + (lane & 31) * 16;
            until<1>(t0 + TQ * 16 - 320);
            lds_wait<1>(C_X * 4, 1);
            T = lds_get(ctl + C_T);
            until<0>(t0 + TQ * 16);
            ST(2);
            v4u d[8];
            const int it = gather<0>(d, T, SPIN, [&](int i, auto A) { return ld4<A.v>(rw, qo + (i & 1) * 2048, (i >> 1) * 4096); });
#pragma unroll
            for (int i = 0; i < 4; ++i) {
                const v4f q = asf(d[2 * i]) + asf(d[2 * i + 1]);
                L.qt[(h0 + i) * 64 + (lane & 31) * 2 + (lane >> 5)] = pk_bf(swap32_add(q[0], q[2]), swap32_add(q[1], q[3]));
            }
            lds_add<C_Q>(1);
            ST(3); STV(14, it);
        }
        const v4u dm = diag_mask(lane);
        const int fo = WS_FP + (lane >> 3) * FPS + (lane & 7) * 16;
        v4u w[32];
        if (wave == 0) {
            const rsrc_t rt = mk(wop + ((kvh * 1024 + rg * 8) * 2048 + (j & 3) * 512), (7 * 128 + 8) * 2048 - (j & 3) * 512);
            const int vo = g * 2048 + n * 16, pace = heavy ? PACE2 : PACE;
            int nx = t0 + (heavy ? slot(4) : slot(3));
            until<1>(nx - 160);
#pragma unroll
            for (int c = 0; c < 8; ++c) {
                until<0>(nx); nx += pace;
#pragma unroll
                for (int a = 0; a < 4; ++a) w[c * 4 + a] = ld4<NT>(rt, vo + (a >> 1) * 8192 + (a & 1) * 256, (c ^ wr) << 18);
            }
            ST(4);
            const int so = fo + (j & 3) * 16 * FPS + (kvh * 16 + rg) * 128;
            asm volatile("" :: "v"(dm), "v"(so));
            lds_wait<0>(C_O * 4, 4);
            v4u xa[4];
#pragma unroll
            for (int i = 0; i < 4; ++i) xa[i] = *(const v4u*)(trow + TAB_O + 4 * i);
            ST(5);
            v4f acc[4] = {};
#pragma unroll
            for (int c = 0; c < 16; ++c) {
                const v4u f = frag(xa[c >> 2][c & 3], dm);
#pragma unroll
                for (int a = 0; a < 2; ++a) acc[(c & 1) * 2 + a] = mfma(f, w[c * 2 + a], acc[(c & 1) * 2 + a]);
            }
            st4(rw, so, tag4(acc[0] + acc[2], T));
            st4(rw, so + 8 * FPS, tag4(acc[1] + acc[3], T));
            lds_add<C_F>(1);
            ST(6);
        } else if (heavy) {
            const int pitch = wave == 2 ? 7168 : 2048;
            const rsrc_t rt = mk(wave == 2 ? w1p + (x * 128 * 7168 + j * 256) : w2p + (grp * 64 * 2048 + (id2 & 7) * 256),
                                 wave == 2 ? 127 * 7168 + 256 : (1792 + 63) * 2048 + 256);
            const int pace = wave == 3 ? PACE2 : PACE;
            int vo[4];
#pragma unroll
            for (int i = 0; i < 4; ++i) vo[i] = (g + 4 * i) * pitch + n * 16;
            int nx = t0 + (wave == 2 ? slot(3) : slot(5));
            until<1>(nx - 160);
#pragma unroll
            for (int c = 0; c < 8; ++c) {
                const int sf = wave == 2 ? (c ^ xr) * 16 * 7168 : ((c >> 2) * 1792 + (c & 3) * 16) * 2048;
                until<0>(nx); nx += pace;
#pragma unroll
                for (int i = 0; i < 4; ++i) w[4 * c + i] = ld4<NT>(rt, vo[i], sf);
            }
            ST(4);
            const int so = wave == 2 ? WS_HP + (j < 14 ? j * 16384 : (j - 14) * 16384 + 512) + x * 1024 + (n >> 3) * 8192 + g * 128 + (n & 7) * 16
                                     : fo + (id2 & 7) * 8 * FPS + (64 + grp) * 128;
            asm volatile("" :: "v"(dm), "v"(so));
            if (wave == 3) lds_wait<0>(C_H * 4, 1);
            ST(5);
            const v4f r = gemv(w, (const v4u*)(trow + (wave == 3 ? TAB_H : b * 32)), dm);
            st4(rw, so, tag4(r, T));
            ST(6);
            if (wave == 2) {
                until<1>(later(clk() + GH * 16, t0 + TH * 16));
                v4u d[8];
                ST(8);
                const int it = gather<2>(d, T, SPIN, [&](int i, auto A) { return ld4<A.v>(rw, WS_HP + grp * 8192 + lane * 16 + (i & 3) * 1024, (i >> 2) * 4096); });
                const v4f h = sum8(d);
                unsigned* t = L.tab + TAB_H + (g & 1) * 4 * TS + (lane >> 5) * 16 + (lane & 7) * 2 + (n >> 3);
#pragma unroll
                for (int e = 0; e < 4; ++e) { const float v = fmaxf(h[e], 0.f); t[e * TS] = pk_bf(v, v); }
                lds_add<C_H>(1);
                ST(9); STV(15, it);
            }
        } else {
            const int oct = (j & 3) * 2 + (wave & 1);
            const int ocol = (oct >> 2) * 1024 + x * 128 + (oct & 1) * 64 + (lane & 7) * 8 + (oct & 2) * 2 + g;
            const int lo = WS_FP + (x * 8 + oct) * FPS + lane * 16;
            const unsigned xo = __builtin_amdgcn_raw_buffer_load_b16(mk(xp, 4096), ocol * 2, 0, 0);
            lds_wait<1>(C_F * 4, 1);
            until<1>(clk() + GS * 16);
            ST(7);
            v4u d[8];
            const int it = gather<3>(d, T, SPIN, [&](int i, auto A) { return ld4<A.v>(rw, lo + (i & 3) * 1024, (i >> 2) * 4096); });
            const v4f s4 = sum8(d);
            float s = swap16_add(swap32_add(s4[0], s4[2]), swap32_add(s4[1], s4[3]));
            s = rbf(s + dpp<ROR8>(s));
            asm volatile("" :: "v"(s), "v"(xo));
            ST(10);
            until<1>(t0 + TF * 16);
            v4u e[4];
            const int iu = gather<4>(e, T, SPIN, [&](int i, auto A) { return ld4<A.v>(rw, (i < 3 || lane < 32 ? lo : lo - 512) + i * 1024, 8192); });
            ST(11);
            if (blk == 255 && wave == 3) st1(rw, lane == 0 ? WS_EP : OOB, T);
            const v4f f4 = (asf(e[0]) + asf(e[1])) + (asf(e[2]) + (lane < 32 ? asf(e[3]) : v4f{0, 0, 0, 0}));
            float f = swap16_add(swap32_add(f4[0], f4[2]), swap32_add(f4[1], f4[3]));
            f = rbf(f + dpp<ROR8>(f));
            const float ga = rbf(f / (1.f + __expf(-f)));
            __builtin_amdgcn_raw_buffer_store_b16((unsigned short)pk_bf(blo(xo) + rbf(s * ga), 0.f), mk(outp, 4096), (lane & 8) ? OOB : ocol * 2, 0, 0);
            ST(12); STV(15, it); STV(13, iu);
        }
    } else {
        const int a = wave - 4;
        const bool odd = n & 1;
        v4u kr[8], vr[8];
        ST0(); BAR();
        GET_T0();
        {
            const int rot = KVC == 2 ? x : 0;
            const size_t kvo = ((size_t)(b * 4 + kvh) * SK + (SK - WIN_KEYS) + j * 16 + ((a ^ rot) & 3) * 512) * 256;
            const rsrc_t rk = mk(kp + kvo, 0x81000), rv = mk(vp + kvo, 0x81000);
            const int ko = (n >> 1) * 256 + (n & 1) * 64 + g * 16, vo = g * 512 + n * 16;
            const int nx = t0 + slot(1) + a * PACE, cx = (rot & 4) << 17;
#pragma unroll
            for (int c = 0; c < 2; ++c) {
                until<1>(nx + c * 4 * PACE);
#pragma unroll
                for (int i = 4 * c; i < 4 * c + 4; ++i) kr[i] = ld4<NT>(rk, ko + (i & 2) * 1024 + (i & 1) * 128, c << 19 ^ cx);
            }
            ST(1);
#pragma unroll
            for (int c = 0; c < 2; ++c) {
                until<1>(nx + (c + 2) * 4 * PACE);
#pragma unroll
                for (int i = 4 * c; i < 4 * c + 4; ++i) vr[i] = ld4<NT>(rv, vo + (i & 2) * 1024 + (i & 1) * 256, c << 19 ^ cx);
            }
            ST(2);
        }
        const int hd = lane & 7, ln = (n >> 3) * 16 + a * 4 + g;
        const int so = WS_OP + kvh * 0x40000 + (ln >> 1) * 16384 + (j * 2 + b) * 256 + (ln & 1) * 128 + hd * 16;
        const int oo = WS_OP + kvh * 0x40000 + rg * 16384 + (lane >> 3) * 512 + a * 128 + hd * 16;
        asm volatile("" :: "v"(so), "v"(oo));
        lds_wait<0>(C_Q * 4, 2);
        const unsigned T = lds_get(ctl + C_T);
        {
            ST(4);
            const v4u qb0 = *(const v4u*)(L.qt + (n >> 1) * 64 + (n & 1) * 16 + g * 4);
            const v4u qb1 = *(const v4u*)(L.qt + (n >> 1) * 64 + 32 + (n & 1) * 16 + g * 4);
            float sc[4];
#pragma unroll
            for (int kg = 0; kg < 4; ++kg) {
                v4f d = mfma(kr[2 * kg], qb0, v4f{0, 0, 0, 0});
                d = mfma(kr[2 * kg + 1], qb1, d);
                float snd = odd ? d[1] : d[2], keep = odd ? d[3] : d[0];
                sc[kg] = rbf(rbf(keep + dpp<XOR1>(snd)) * 0.08838834764831845f);
            }
            float mx = fmx(fmx(sc[0], sc[1]), fmx(sc[2], sc[3]));
            mx = fmx(mx, dpp<XOR1>(mx)); mx = swap16_max(mx); mx = swap32_max(mx);
            float pe[4]; v4u pa;
#pragma unroll
            for (int kg = 0; kg < 4; ++kg) { pe[kg] = __expf(sc[kg] - mx); pa[kg] = pk_bf(0.f, pe[kg]) >> (odd ? 0 : 16); }
            float ls = (pe[0] + pe[1]) + (pe[2] + pe[3]);
            ls += dpp<XOR1>(ls); ls = swap16_add(ls, ls); ls = swap32_add(ls, ls);
            if (lane < 16 && !odd) *(v2f*)&L.ml[n >> 1][a][0] = v2f{mx, ls};
            ST(5);
            v4f oa[2], ob[2];
#pragma unroll
            for (int d = 0; d < 8; ++d) {
                v4u vt;
#pragma unroll
                for (int e = 0; e < 4; ++e) vt[e] = __builtin_amdgcn_perm(vr[2 * e + 1][d >> 1], vr[2 * e][d >> 1], (d & 1) ? 0x07060302u : 0x05040100u);
                const v4f r = mfma(pa, vt, v4f{0, 0, 0, 0});
                oa[d >> 2][d & 3] = r[0] + r[1]; ob[d >> 2][d & 3] = r[2] + r[3];
            }
            float* ow = L.ow[a] + g * 2 * OWS + n * 4;
            *(v4f*)(ow) = oa[0]; *(v4f*)(ow + 64) = oa[1]; *(v4f*)(ow + OWS) = ob[0]; *(v4f*)(ow + OWS + 64) = ob[1];
            lds_add<C_M>(1);
            ST(6);
        }
        {
            lds_wait<0>(C_M * 4, 4);
            const v4f m0 = *(const v4f*)&L.ml[hd][0][0], m1 = *(const v4f*)&L.ml[hd][2][0];
            v4f o4[4];
#pragma unroll
            for (int w = 0; w < 4; ++w) o4[w] = *(const v4f*)(L.ow[w] + hd * OWS + (ln & 1) * 64 + (ln >> 1) * 4);
            const float M = fmx(fmx(m0[0], m0[2]), fmx(m1[0], m1[2]));
            const float c0 = __expf(m0[0] - M), c1 = __expf(m0[2] - M), c2 = __expf(m1[0] - M), c3 = __expf(m1[2] - M);
            const float Ls = (m0[1] * c0 + m0[3] * c1) + (m1[1] * c2 + m1[3] * c3);
            const v4f o = ((o4[0] * c0 + o4[1] * c1) + (o4[2] * c2 + o4[3] * c3)) * __builtin_amdgcn_rcpf(Ls);
            const unsigned lse = f2u(M + __logf(Ls)), p0 = pk_bf(o[0], o[1]), p1 = pk_bf(o[2], o[3]);
            const unsigned e0 = __builtin_amdgcn_perm(lse, T, 0x05010400u), e1 = __builtin_amdgcn_perm(lse, T, 0x07030602u);
            st4(rw, so, v4u{__builtin_amdgcn_perm(p0, e0, 0x05040100u), __builtin_amdgcn_perm(p0, e0, 0x07060302u),
                            __builtin_amdgcn_perm(p1, e1, 0x05040100u), __builtin_amdgcn_perm(p1, e1, 0x07060302u)});
            const int tg = later(clk() + GO * 16, t0 + TO * 16);
            ST(7);
            until<1>(tg);
            ST(8);
        }
        {
            v4u d[4];
            const int it = gather<1>(d, T, SPIN, [&](int i, auto A) { return ld4<A.v>(rw, oo, i * 4096); });
            ST(9);
            float l[4];
#pragma unroll
            for (int i = 0; i < 4; ++i)
                l[i] = u2f(__builtin_amdgcn_perm(__builtin_amdgcn_perm(d[i][3], d[i][2], 0x0501u), __builtin_amdgcn_perm(d[i][1], d[i][0], 0x0501u), 0x05040100u));
            float M = fmx(fmx(l[0], l[1]), fmx(l[2], l[3]));
            M = fmx(M, dpp<ROR8>(M)); M = swap16_max(M); M = swap32_max(M);
            v4f acc = {0, 0, 0, 0};
#pragma unroll
            for (int i = 0; i < 4; ++i) { l[i] = __expf(l[i] - M); acc += __builtin_bit_cast(v4f, d[i] & 0xffff0000u) * l[i]; }
            float Ls = (l[0] + l[1]) + (l[2] + l[3]);
            Ls += dpp<ROR8>(Ls); Ls = swap16_add(Ls, Ls); Ls = swap32_add(Ls, Ls);
            float r = swap16_add(swap32_add(acc[0], acc[2]), swap32_add(acc[1], acc[3]));
            r += dpp<ROR8>(r);
            r *= __builtin_amdgcn_rcpf(Ls);
            L.tab[TAB_O + ((a >> 1) * 4 + g) * TS + (hd ^ wr) * 2 + (a & 1)] = pk_bf(r, r);
            lds_add<C_O>(1);
            ST(10); STV(15, it);
        }
        asm volatile("" :: "v"(pf));
    }
}

extern "C" void kernel_launch(void* const* d_in, const int* in_sizes, int n_in, void* d_out, int out_size, void* d_ws, size_t ws_size,
                              hipStream_t stream) {
    fused_decode<<<256, 512, 0, stream>>>(d_in[0], d_in[1], d_in[2], d_in[3], d_in[4], d_in[6], d_in[7], d_out, (char*)d_ws);
}
```

```cpp
#define PACE 276
#define PACE2 195
#define GAP4 0
#define GAP5 43
#define TO 690
#define TH 700
#define KVC 1
#define TF 875
#define TQ 271
#define CODE_BYTES 18284
#include <hip/hip_runtime.h>
#include <hip/hip_bf16.h>
#include <stdint.h>

typedef unsigned v4u __attribute__((ext_vector_type(4)));
typedef unsigned v16u __attribute__((ext_vector_type(16)));
typedef unsigned v2u __attribute__((ext_vector_type(2)));
typedef float v4f __attribute__((ext_vector_type(4)));
typedef float v2f __attribute__((ext_vector_type(2)));
typedef __bf16 v8bf __attribute__((ext_vector_type(8)));
typedef __bf16 v2bf __attribute__((ext_vector_type(2)));
typedef unsigned long long u64;
typedef __amdgpu_buffer_rsrc_t rsrc_t;

#define GO 50
#define GH 45
#define GAP3 0
#define PACE3 PACE2
#define FST 16
#define FW 8
#define T1ADJ 0
#define BARL 0
#define DLQ 0
#define DLO 0
#define DLH 0
#define SAUX 16
#define CONT 0
#define NOST 0
#define SB() __builtin_amdgcn_sched_barrier(0)
#define CB() asm volatile("" ::: "memory")
#define BAR() do { CB(); SB(); __builtin_amdgcn_s_barrier(); SB(); CB(); } while (0)
#define VMCNT(n) do { SB(); asm volatile("s_waitcnt vmcnt(%0)" :: "n"(n) : "memory"); SB(); } while (0)

constexpr int SK = 32768, WIN_KEYS = 4096;
constexpr int NT = 2, SC1 = 16;
constexpr int OOB = 0x40000000;
#define LSH 0
constexpr int SPIN = 1 << (22 - LSH), LSPIN = 1 << (27 - LSH);
constexpr int WS_EP = 0, WS_QP = 0x10000, WS_HP = 0x30000, WS_OP = 0x70000, WS_FP = 0x170000, WS_END = 0x230000;
constexpr int FPS = 93 * 128;
enum { C_X = 1, C_Q = 3, C_M = 4, C_O = 5, C_H = 6, C_T = 7, C_F = 8 };
constexpr int TS = 132;
constexpr int TAB_H = 64, TAB_O = 96;
constexpr int OWS = 132;

struct alignas(16) Lds {
    unsigned ctl[16];
    unsigned tab[8 * TS];
    unsigned qt[512];
    float ml[8][4][2];
    float ow[4][8 * OWS];
};

__device__ __forceinline__ rsrc_t mk(const void* p, int n) { return __builtin_amdgcn_make_buffer_rsrc((void*)p, 0, n, 0x00020000); }
template <int AUX> __device__ __forceinline__ v4u ld4(rsrc_t r, int off, int so = 0) { return __builtin_amdgcn_raw_buffer_load_b128(r, off, so, AUX); }
__device__ __forceinline__ void st4(rsrc_t r, int off, v4u v) { if (NOST) { asm volatile("" :: "v"(v)); return; } __builtin_amdgcn_raw_buffer_store_b128(v, r, off, 0, SAUX); }
__device__ __forceinline__ void st1(rsrc_t r, int off, unsigned v) { __builtin_amdgcn_raw_buffer_store_b32(v, r, off, 0, SC1); }
__device__ __forceinline__ float u2f(unsigned u) { return __uint_as_float(u); }
__device__ __forceinline__ unsigned f2u(float f) { return __float_as_uint(f); }
__device__ __forceinline__ unsigned pk_bf(float lo, float hi) { return __builtin_bit_cast(unsigned, __builtin_convertvector(v2f{lo, hi}, v2bf)); }
__device__ __forceinline__ float rbf(float f) { return u2f(pk_bf(0.f, f)); }
__device__ __forceinline__ float blo(unsigned u) { return u2f(u << 16); }
__device__ __forceinline__ float bhi(unsigned u) { return u2f(u & 0xffff0000u); }
template <int CTRL> __device__ __forceinline__ unsigned dppu(unsigned v) { return __builtin_amdgcn_update_dpp(0u, v, CTRL, 0xf, 0xf, true); }
template <int CTRL> __device__ __forceinline__ float dpp(float v) { return u2f(dppu<CTRL>(f2u(v))); }
__device__ __forceinline__ v4f mfma(v4u a, v4u b, v4f c) { return __builtin_amdgcn_mfma_f32_16x16x32_bf16(__builtin_bit_cast(v8bf, a), __builtin_bit_cast(v8bf, b), c, 0, 0, 0); }
__device__ __forceinline__ float fmx(float a, float b) { return __builtin_amdgcn_fmed3f(a, b, __builtin_inff()); }
__device__ __forceinline__ float swap32_add(float a, float b) { auto r = __builtin_amdgcn_permlane32_swap(f2u(a), f2u(b), false, false); return u2f(r[0]) + u2f(r[1]); }
__device__ __forceinline__ float swap16_add(float a, float b) { auto r = __builtin_amdgcn_permlane16_swap(f2u(a), f2u(b), false, false); return u2f(r[0]) + u2f(r[1]); }
__device__ __forceinline__ float swap32_max(float a) { auto r = __builtin_amdgcn_permlane32_swap(f2u(a), f2u(a), false, false); return fmx(u2f(r[0]), u2f(r[1])); }
__device__ __forceinline__ float swap16_max(float a) { auto r = __builtin_amdgcn_permlane16_swap(f2u(a), f2u(a), false, false); return fmx(u2f(r[0]), u2f(r[1])); }
constexpr int XOR1 = 0xB1, XOR2 = 0x4E, HMIR = 0x141, ROR8 = 0x128, ROR4 = 0x124, ROR2 = 0x122, BCAST = 0x150;

__device__ __forceinline__ unsigned lds_get(unsigned* p) { return __builtin_amdgcn_readfirstlane(__hip_atomic_load(p, __ATOMIC_RELAXED, __HIP_MEMORY_SCOPE_WORKGROUP)); }
template <bool SLEEP> __device__ __forceinline__ int lds_wait(unsigned addr, int v) {
    unsigned t, c; int s;
    if (SLEEP)
        asm volatile("s_mov_b32 %2, %5\n1:\n\tds_read_b32 %0, %3\n\ts_waitcnt lgkmcnt(0)\n\tv_readfirstlane_b32 %1, %0\n\ts_cmp_ge_i32 %1, %4\n\t"
                     "s_cbranch_scc1 2f\n\ts_sleep 1\n\ts_sub_u32 %2, %2, 1\n\ts_cbranch_scc0 1b\n2:"
                     : "=&v"(t), "=&s"(s), "=&s"(c) : "v"(addr), "s"(v), "n"(LSPIN) : "memory", "scc");
    else
        asm volatile("s_mov_b32 %2, %5\n1:\n\tds_read_b32 %0, %3\n\ts_waitcnt lgkmcnt(0)\n\tv_readfirstlane_b32 %1, %0\n\ts_cmp_ge_i32 %1, %4\n\t"
                     "s_cbranch_scc1 2f\n\ts_sub_u32 %2, %2, 1\n\ts_cbranch_scc0 1b\n2:"
                     : "=&v"(t), "=&s"(s), "=&s"(c) : "v"(addr), "s"(v), "n"(LSPIN) : "memory", "scc");
    return s;
}
template <int W> __device__ __forceinline__ void lds_add(unsigned v) {
    asm volatile("s_mov_b64 exec, 1\n\tds_add_u32 %0, %1 offset:%2\n\ts_mov_b64 exec, -1" :: "v"(0), "v"(v), "n"(W * 4) : "memory");
}
__device__ __forceinline__ v4u tag4(v4f v, unsigned T) {
    v4u r;
#pragma unroll
    for (int i = 0; i < 4; ++i) r[i] = __builtin_amdgcn_perm(f2u(v[i]), T, 0x07060500u + i);
    return r;
}
__device__ __forceinline__ v4f asf(v4u d) { return __builtin_bit_cast(v4f, d & 0xffffff00u); }
__device__ __forceinline__ bool any(bool p) { return __builtin_amdgcn_ballot_w64(p) != 0; }
__device__ __forceinline__ unsigned tag_of(v4u d) {
    return __builtin_amdgcn_perm(__builtin_amdgcn_perm(d[3], d[2], 0x0400u), __builtin_amdgcn_perm(d[1], d[0], 0x0400u), 0x05040100u);
}
template <int N> __device__ __forceinline__ void anyval(v4u (&d)[N]) {
#pragma unroll
    for (int i = 0; i < N; ++i) asm volatile("" : "=v"(d[i]));
}
__device__ __forceinline__ int clk() { int t; asm volatile("s_memrealtime vcc\n\ts_waitcnt lgkmcnt(0)\n\ts_lshl_b32 %0, vcc_lo, 4" : "=s"(t) :: "vcc", "scc", "memory"); return t; }
template <bool SLEEP> __device__ __forceinline__ void until(int t) {
    SB();
    if (SLEEP)
        asm volatile("1:\n\ts_memrealtime vcc\n\ts_waitcnt lgkmcnt(0)\n\ts_lshl_b32 vcc_lo, vcc_lo, 4\n\ts_sub_u32 vcc_lo, %0, vcc_lo\n\t"
                     "s_cmpk_gt_u32 vcc_lo, 0xffff\n\ts_cbranch_scc1 3f\n"
                     "2:\n\ts_sub_u32 vcc_lo, vcc_lo, 64\n\ts_cbranch_scc1 1b\n\ts_sleep 1\n\ts_branch 2b\n3:" :: "s"(t - 1) : "vcc", "scc", "memory");
    else
        asm volatile("1:\n\ts_memrealtime vcc\n\ts_waitcnt lgkmcnt(0)\n\ts_lshl_b32 vcc_lo, vcc_lo, 4\n\ts_sub_u32 vcc_lo, %0, vcc_lo\n\t"
                     "s_cmpk_lt_u32 vcc_lo, 0xffff\n\ts_cbranch_scc1 1b" :: "s"(t - 1) : "vcc", "scc", "memory");
    SB();
}
#define EX(i) "s_mov_b64 exec, %[m" #i "]\n\t"
#define LD(i, o) "buffer_load_dwordx4 %[d" #i "], %[vo], %[rs], %[so] offen offset:" #o " sc1\n\t"
#define UP "s_add_u32 %[so], %[so], 0x1000\n\t"
#define FIN "s_mov_b64 exec, -1\n\ts_waitcnt vmcnt(0)"
#define D4(a, b, c, e) [d##a] "+v"(d[a]), [d##b] "+v"(d[b]), [d##c] "+v"(d[c]), [d##e] "+v"(d[e])
#define M4(a, b, c, e) [m##a] "s"(m[a]), [m##b] "s"(m[b]), [m##c] "s"(m[c]), [m##e] "s"(m[e])
__device__ __forceinline__ void reload(v4u (&d)[4], const u64 (&m)[4], int vo, v4u rs) {
    int so = 0;
    asm volatile(EX(0) LD(0, 0) UP EX(1) LD(1, 0) UP EX(2) LD(2, 0) UP EX(3) LD(3, 0) FIN
                 : D4(0, 1, 2, 3), [so] "+&s"(so) : M4(0, 1, 2, 3), [vo] "v"(vo), [rs] "s"(rs) : "memory", "scc");
}
__device__ __forceinline__ void reload(v4u (&d)[8], const u64 (&m)[8], int vo, v4u rs) {
    int so = 0;
    asm volatile(EX(0) LD(0, 0) EX(1) LD(1, 1024) EX(2) LD(2, 2048) EX(3) LD(3, 3072) UP EX(4) LD(4, 0) EX(5) LD(5, 1024) EX(6) LD(6, 2048) EX(7) LD(7, 3072) FIN
                 : D4(0, 1, 2, 3), D4(4, 5, 6, 7), [so] "+&s"(so) : M4(0, 1, 2, 3), M4(4, 5, 6, 7), [vo] "v"(vo), [rs] "s"(rs) : "memory", "scc");
}
#define P2(k, a, b) EX(k) LD(a, 0) LD(b, 2048)
__device__ __forceinline__ void reload(v4u (&d)[16], const u64 (&m)[8], int vo, v4u rs) {
    int so = 0;
    asm volatile(P2(0, 0, 1) UP P2(1, 2, 3) UP P2(2, 4, 5) UP P2(3, 6, 7) UP P2(4, 8, 9) UP P2(5, 10, 11) UP P2(6, 12, 13) UP P2(7, 14, 15) FIN
                 : D4(0, 1, 2, 3), D4(4, 5, 6, 7), D4(8, 9, 10, 11), D4(12, 13, 14, 15), [so] "+&s"(so)
                 : M4(0, 1, 2, 3), M4(4, 5, 6, 7), [vo] "v"(vo), [rs] "s"(rs) : "memory", "scc");
}
template <int N> __device__ __forceinline__ int gather(v4u (&d)[N], unsigned T, rsrc_t rw, v4u rs, int vo) {
    constexpr int M = N == 16 ? 8 : N, K = N == 8 ? 4 : 1;
    int it = 0;
    u64 m[M];
    auto check = [&] {
        u64 more = 0;
#pragma unroll
        for (int i = 0; i < M; ++i) {
            m[i] = __builtin_amdgcn_ballot_w64(tag_of(d[i * (N / M)]) != T);
            if (N != M) m[i] |= __builtin_amdgcn_ballot_w64(tag_of(d[i * (N / M) + 1]) != T);
            more |= m[i];
        }
        return more;
    };
    if constexpr (N == 16) {
        anyval(d);
#pragma unroll
        for (int i = 0; i < M; ++i) { m[i] = ~0ull; asm volatile("" : "+s"(m[i])); }
    } else {
        CB();
#pragma unroll
        for (int i = 0; i < N; ++i) d[i] = ld4<SC1>(rw, vo + i % K * 1024, i / K * 4096);
        if (__builtin_expect(check() == 0, 1)) return 0;
        ++it;
    }
#pragma unroll 1
    do { SB(); reload(d, m, vo, rs); SB(); } while (check() != 0 && ++it < SPIN);
    return it;
}
__device__ __forceinline__ v4f sum8(const v4u (&d)[8]) {
    return ((asf(d[0]) + asf(d[1])) + (asf(d[2]) + asf(d[3]))) + ((asf(d[4]) + asf(d[5])) + (asf(d[6]) + asf(d[7])));
}
#define ST(i)
#define STV(i, v) (void)(v)
__device__ __forceinline__ v4u diag_mask(int lane) {
    const int e = lane & 7; const unsigned h = (e & 1) ? 0xffff0000u : 0x0000ffffu;
    return v4u{(e >> 1) == 0 ? h : 0u, (e >> 1) == 1 ? h : 0u, (e >> 1) == 2 ? h : 0u, (e >> 1) == 3 ? h : 0u};
}
__device__ __forceinline__ v4u frag(unsigned xa, v4u m) { return v4u{xa & m[0], xa & m[1], xa & m[2], xa & m[3]}; }
constexpr int slot(int k) { return (k <= 4 ? k * 8 * PACE : 32 * PACE + (k - 4) * 8 * PACE2) + 16 * ((k >= 3) * GAP3 + (k >= 4) * GAP4 + (k >= 5) * GAP5); }
__device__ __forceinline__ int later(int a, int b) { return a - b > 0 ? a : b; }
__device__ __forceinline__ v4f gemv(const v4u (&w)[32], const v4u* tr, v4u dm) {
    v4u xa[8], f[32];
#pragma unroll
    for (int i = 0; i < 8; ++i) xa[i] = tr[i];
    v4f a0 = {0, 0, 0, 0}, a1 = {0, 0, 0, 0};
#pragma unroll
    for (int i = 0; i < 4; ++i) f[i] = frag(xa[0][i], dm);
    SB();
#pragma unroll
    for (int i = 0; i < 32; ++i) {
        if (i & 1) a1 = mfma(f[i], w[i], a1); else a0 = mfma(f[i], w[i], a0);
        if (i < 28) f[i + 4] = frag(xa[(i + 4) >> 2][i & 3], dm);
        SB();
    }
    return a0 + a1;
}
#define SWX 1
#define SWO 1

__global__ void __launch_bounds__(512, 2)
fused_decode(const void*, const void*, const void*, const void*, const void*, const void*, const void*, void*, void*) {
    __shared__ Lds L;
    const int lane = threadIdx.x & 63;
    const int wave = __builtin_amdgcn_readfirstlane(threadIdx.x >> 6);
    const int blk = blockIdx.x, x = blk & 7, j = blk >> 3, kvh = x >> 1, b = x & 1;
    v16u ka; u64 kw;
    asm volatile("s_load_dwordx16 %0, %2, 0x0\n\ts_load_dwordx2 %1, %2, 0x40"
                 : "=&s"(ka), "=&s"(kw) : "s"(__builtin_amdgcn_kernarg_segment_ptr()));
#define ST0()
    unsigned pf; u64 pc;
    {
        asm volatile("s_getpc_b64 %1\n\tv_cmp_gt_u32 vcc, %3, %2\n\ts_mov_b64 exec, vcc\n\ts_cbranch_execz 1f\n\tglobal_load_dword %0, %2, %1\n1:\n\ts_mov_b64 exec, -1"
                     : "=v"(pf), "=&s"(pc) : "v"((threadIdx.x ^ 256) * 128), "s"(CODE_BYTES) : "vcc");
    }
    unsigned* ctl = L.ctl;
    const int g = lane >> 4, n = lane & 15;
    const int cb = (j & 16) * 2 + ((j ^ b * 16) & 31), xr = SWX == 1 ? kvh * 2 : SWX == 2 ? x : 0;
    constexpr int QSH = CONT ? 20 : 17;
    int qoff = kvh * (CONT ? 32 : 256) * 8192 + cb * 128, qs = xr << QSH, qv[8];
#pragma unroll
    for (int i = 0; i < 8; ++i) qv[i] = g * 8192 + n * 8 + (i >> 1) * 32768 + (i & 1) * ((CONT ? 16 : 128) * 8192);
    int t0; unsigned T;
    asm volatile("s_cmp_eq_u32 %5, 1\n\ts_waitcnt lgkmcnt(0)\n\ts_cbranch_scc1 1f\n\ts_memrealtime vcc\n\ts_waitcnt lgkmcnt(0)\n1:\n\ts_lshl_b32 %4, vcc_lo, 4"
                 : "+s"(ka), "+s"(kw), "+s"(qoff), "+s"(qs), "=s"(t0)
                 : "s"(wave), "v"(qv[0]), "v"(qv[1]), "v"(qv[2]), "v"(qv[3]), "v"(qv[4]), "v"(qv[5]), "v"(qv[6]), "v"(qv[7]) : "vcc", "scc", "memory");
    auto arg = [&](int i) { return (char*)((u64)ka[2 * i] | (u64)ka[2 * i + 1] << 32); };
    const char *xp = arg(0), *kp = arg(1), *vp = arg(2), *wqp = arg(3), *wop = arg(4), *w1p = arg(5), *w2p = arg(6);
    char *outp = arg(7), *ws = (char*)kw;
    const int rg = b * 8 + (j >> 2), wr = SWO ? kvh * 2 : 0;
    const bool heavy = j < 28;
    const rsrc_t rw = mk(ws, WS_END);
    const v4u rs = {(unsigned)kw, (unsigned)(kw >> 32) & 0xffffu, (unsigned)WS_END, 0x00020000u};
    const unsigned* trow = L.tab + (((lane >> 3) & 1) * 4 + g) * TS;
    if (__builtin_expect(wave == 1, 1)) {
        const rsrc_t rt = mk(wqp + qoff, CONT ? (7 * 128 + 31) * 8192 + 128 : 8192 * 256);
        v2u h[64];
#pragma unroll
        for (int i = 0; i < 8; ++i) h[i] = __builtin_amdgcn_raw_buffer_load_b64(rt, qv[i], qs, NT);
        SB(); t0 = clk() - T1ADJ;
        ST0(); ST(13);
        if (!BARL) BAR();
#pragma unroll
        for (int c = 1; c < 8; ++c) {
            until<0>(t0 + c * PACE);
#pragma unroll
            for (int i = 0; i < 8; ++i) h[8 * c + i] = __builtin_amdgcn_raw_buffer_load_b64(rt, qv[i], qs ^ c << QSH, NT);
            if (BARL == c) BAR();
        }
        ST(4);
        const v4u dm = diag_mask(lane);
        const int so = (g & 1) ? OOB : WS_QP + (cb >> 1) * 4096 + kvh * 1024 + (g >> 1) * 512 + (cb & 1) * 256 + n * 16;
        asm volatile("" :: "v"(dm), "v"(so));
        lds_wait<0>(C_X * 4, 1);
        ST(5);
        T = lds_get(ctl + C_T);
        v4u w[32];
#pragma unroll
        for (int i = 0; i < 32; ++i) w[i] = v4u{h[2 * i][0], h[2 * i][1], h[2 * i + 1][0], h[2 * i + 1][1]};
        v4f q = gemv(w, (const v4u*)(trow + (lane & 4) * 8), dm);
#pragma unroll
        for (int e = 0; e < 4; ++e) q[e] = swap16_add(q[e], q[e]);
        if (DLQ && blk % 37 == 5) until<0>(t0 + DLQ * 16);
        st4(rw, so, tag4(q, T));
        ST(6);
        const int qo = WS_QP + kvh * 8 * 4096 + (lane >> 5) * 1024 + b * 512 + (lane & 31) * 16;
        asm volatile("" :: "v"(qo));
        until<1>(t0 + TQ * 16);
        ST(2);
        v4u d[16];
        const int it = gather(d, T, rw, rs, qo);
#pragma unroll
        for (int i = 0; i < 8; ++i) {
            const v4f q = asf(d[2 * i]) + asf(d[2 * i + 1]);
            L.qt[i * 64 + (lane & 31) * 2 + (lane >> 5)] = pk_bf(swap32_add(q[0], q[2]), swap32_add(q[1], q[3]));
        }
        lds_add<C_Q>(1);
        ST(3); STV(14, it);
    } else if (__builtin_expect(wave < 4, 1)) {
        const int id2 = x * 28 + j, grp = id2 >> 3;
        if (wave == 2) {
            const rsrc_t rx = mk(xp, 4096);
            v4u xr4[4];
#pragma unroll
            for (int t = 0; t < 4; ++t) xr4[t] = CONT ? ld4<0>(rx, (lane >> 5) * 2048 + (lane & 14) * 128 + (lane & 16) * 2 + (lane & 1) * 16, (t ^ kvh) * 64)
                                                     : ld4<0>(rx, (lane >> 5) * 2048 + (lane & 31) * 16, (t ^ kvh) * 512);
            const unsigned ep = __builtin_amdgcn_raw_buffer_load_b32(rw, WS_EP, 0, SC1);
            ST0(); BAR();
            float xv[32];
#pragma unroll
            for (int t = 0; t < 4; ++t)
#pragma unroll
                for (int e = 0; e < 4; ++e) { xv[t * 8 + 2 * e] = blo(xr4[t][e]); xv[t * 8 + 2 * e + 1] = bhi(xr4[t][e]); }
            v2f sp[4];
#pragma unroll
            for (int t = 0; t < 4; ++t) sp[t] = (v2f{xv[8 * t], xv[8 * t + 1]} + v2f{xv[8 * t + 2], xv[8 * t + 3]}) + (v2f{xv[8 * t + 4], xv[8 * t + 5]} + v2f{xv[8 * t + 6], xv[8 * t + 7]});
            const v2f s2 = (sp[0] + sp[1]) + (sp[2] + sp[3]);
            float s = s2[0] + s2[1];
            s += dpp<XOR1>(s); s += dpp<XOR2>(s); s += dpp<HMIR>(s); s += dpp<ROR8>(s); s = swap16_add(s, s);
            const float mean = s * (1.0f / 1024);
#pragma unroll
            for (int t = 0; t < 4; ++t) {
                sp[t] = v2f{0, 0};
#pragma unroll
                for (int i = 4 * t; i < 4 * t + 4; ++i) { const v2f d = v2f{xv[2 * i], xv[2 * i + 1]} - mean; sp[t] = __builtin_elementwise_fma(d, d, sp[t]); }
            }
            const v2f q2 = (sp[0] + sp[1]) + (sp[2] + sp[3]);
            float q = q2[0] + q2[1];
            q += dpp<XOR1>(q); q += dpp<XOR2>(q); q += dpp<HMIR>(q); q += dpp<ROR8>(q); q = swap16_add(q, q);
            const float mb = rbf(mean), den = rbf(sqrtf(rbf(rbf(q * (1.0f / 1024)) + 1e-5f)));
            unsigned* t = L.tab + (lane >> 5) * 4 * TS + ((lane & 31) * 2 ^ xr * 4);
#pragma unroll
            for (int e = 0; e < 4; ++e) {
                const float v = rbf(xv[e] - mb) / den, w = rbf(xv[e + 4] - mb) / den;
                *(v2u*)(t + e * TS) = v2u{pk_bf(v, v), pk_bf(w, w)};
            }
            T = __builtin_amdgcn_readfirstlane(ep) + 0x01010101u;
            if (lane == 0) ctl[C_T] = T;
            lds_add<C_X>(1);
            ST(1);
        } else {
            if (lane < 16) ctl[lane] = 0;
            ST0();
            asm volatile("s_waitcnt lgkmcnt(0)" ::: "memory");
            BAR();
        }
        if (wave != 2) {
            until<1>(t0 + 250 * 16);
            lds_wait<1>(C_X * 4, 1);
            T = lds_get(ctl + C_T);
        }
        const v4u dm = diag_mask(lane);
        const int fo = WS_FP + (lane >> 3) * FPS + (lane & 7) * 16;
        v4u w[32];
        if (wave == 0) {
            const rsrc_t rt = mk(wop + ((kvh * 1024 + rg * 8) * 2048 + (j & 3) * 512), (7 * 128 + 8) * 2048 - (j & 3) * 512);
            const int vo = g * 2048 + n * 16, pace = heavy ? PACE2 : PACE;
            int nx = t0 + (heavy ? slot(4) : slot(3));
            until<1>(nx - 160);
#pragma unroll
            for (int c = 0; c < 8; ++c) {
                until<0>(nx); nx += pace;
#pragma unroll
                for (int a = 0; a < 4; ++a) w[c * 4 + a] = ld4<NT>(rt, vo + (a >> 1) * 8192 + (a & 1) * 256, (c ^ wr) << 18);
            }
            ST(4);
            const int so = fo + (j & 3) * 16 * FPS + (kvh * 16 + rg) * 128;
            asm volatile("" :: "v"(dm), "v"(so));
            lds_wait<0>(C_O * 4, 4);
            v4u xa[4];
#pragma unroll
            for (int i = 0; i < 4; ++i) xa[i] = *(const v4u*)(trow + TAB_O + 4 * i);
            ST(5);
            v4f acc[4] = {};
#pragma unroll
            for (int c = 0; c < 16; ++c) {
                const v4u f = frag(xa[c >> 2][c & 3], dm);
#pragma unroll
                for (int a = 0; a < 2; ++a) acc[(c & 1) * 2 + a] = mfma(f, w[c * 2 + a], acc[(c & 1) * 2 + a]);
            }
            st4(rw, so, tag4(acc[0] + acc[2], T));
            st4(rw, so + 8 * FPS, tag4(acc[1] + acc[3], T));
            ST(6);
        } else if (heavy) {
            const int pitch = wave == 2 ? 7168 : 2048;
            const rsrc_t rt = mk(wave == 2 ? w1p + (x * (CONT ? 16 : 128) * 7168 + j * 256) : w2p + (grp * 64 * 2048 + (id2 & 7) * 256),
                                 wave == 2 ? (CONT ? 7 * 128 + 15 : 127) * 7168 + 256 : (1792 + 63) * 2048 + 256);
            const int pace = wave == 3 ? PACE3 : PACE;
            int vo[4];
#pragma unroll
            for (int i = 0; i < 4; ++i) vo[i] = (g + 4 * i) * pitch + n * 16;
            int nx = t0 + (wave == 2 ? slot(3) : slot(5));
            until<1>(nx - 160);
#pragma unroll
            for (int c = 0; c < 8; ++c) {
                const int sf = wave == 2 ? (c ^ xr) * (CONT ? 128 : 16) * 7168 : ((c >> 2) * 1792 + (c & 3) * 16) * 2048;
                until<0>(nx); nx += pace;
#pragma unroll
                for (int i = 0; i < 4; ++i) w[4 * c + i] = ld4<NT>(rt, vo[i], sf);
            }
            ST(4);
            const int so = wave == 2 ? WS_HP + (j < 14 ? j * 16384 : (j - 14) * 16384 + 512) + x * 1024 + (n >> 3) * 8192 + g * 128 + (n & 7) * 16
                                     : fo + (id2 & 7) * 8 * FPS + (64 + grp) * 128;
            asm volatile("" :: "v"(dm), "v"(so));
            if (wave == 3) lds_wait<0>(C_H * 4, 1);
            ST(5);
            const v4f r = gemv(w, (const v4u*)(trow + (wave == 3 ? TAB_H : b * 32)), dm);
            if (DLH && wave == 2 && blk % 37 == 5) until<0>(t0 + DLH * 16);
            st4(rw, so, tag4(r, T));
            ST(6);
            if (wave == 2) {
                until<1>(later(clk() + GH * 16, t0 + TH * 16));
                v4u d[8];
                ST(8);
                const int it = gather(d, T, rw, rs, WS_HP + grp * 8192 + lane * 16);
                const v4f h = sum8(d);
                unsigned* t = L.tab + TAB_H + (g & 1) * 4 * TS + (lane >> 5) * 16 + (lane & 7) * 2 + (n >> 3);
#pragma unroll
                for (int e = 0; e < 4; ++e) { const float v = fmaxf(h[e], 0.f); t[e * TS] = pk_bf(v, v); }
                lds_add<C_H>(1);
                ST(9); STV(14, it);
            }
        }
    } else {
        const int a = wave - 4;
        const bool odd = n & 1;
        v4u kr[8], vr[8];
        ST0(); BAR();
        {
            const int rot = KVC == 2 ? x : 0;
            const size_t kvo = ((size_t)(b * 4 + kvh) * SK + (SK - WIN_KEYS) + j * 16 + ((a ^ rot) & 3) * 512) * 256;
            const rsrc_t rk = mk(kp + kvo, 0x81000), rv = mk(vp + kvo, 0x81000);
            const int ko = (n >> 1) * 256 + (n & 1) * 64 + g * 16, vo = g * 512 + n * 16;
            const int nx = t0 + slot(1) + a * PACE, cx = (rot & 4) << 17;
#pragma unroll
            for (int c = 0; c < 2; ++c) {
                until<1>(nx + c * 4 * PACE);
#pragma unroll
                for (int i = 4 * c; i < 4 * c + 4; ++i) kr[i] = ld4<NT>(rk, ko + (i & 2) * 1024 + (i & 1) * 128, c << 19 ^ cx);
            }
            ST(1);
#pragma unroll
            for (int c = 0; c < 2; ++c) {
                until<1>(nx + (c + 2) * 4 * PACE);
#pragma unroll
                for (int i = 4 * c; i < 4 * c + 4; ++i) vr[i] = ld4<NT>(rv, vo + (i & 2) * 1024 + (i & 1) * 256, c << 19 ^ cx);
            }
            ST(2);
        }
        const int hd = lane & 7, ln = (n >> 3) * 16 + a * 4 + g;
        const int so = WS_OP + kvh * 0x40000 + (ln >> 1) * 16384 + (j * 2 + b) * 256 + (ln & 1) * 128 + hd * 16;
        const int oo = WS_OP + kvh * 0x40000 + rg * 16384 + (lane >> 3) * 512 + a * 128 + hd * 16;
        asm volatile("" :: "v"(so), "v"(oo));
        lds_wait<0>(C_Q * 4, 1);
        T = lds_get(ctl + C_T);
        {
            ST(4);
            const v4u qb0 = *(const v4u*)(L.qt + (n >> 1) * 64 + (n & 1) * 16 + g * 4);
            const v4u qb1 = *(const v4u*)(L.qt + (n >> 1) * 64 + 32 + (n & 1) * 16 + g * 4);
            float sc[4];
#pragma unroll
            for (int kg = 0; kg < 4; ++kg) {
                v4f d = mfma(kr[2 * kg], qb0, v4f{0, 0, 0, 0});
                d = mfma(kr[2 * kg + 1], qb1, d);
                float snd = odd ? d[1] : d[2], keep = odd ? d[3] : d[0];
                sc[kg] = rbf(rbf(keep + dpp<XOR1>(snd)) * 0.08838834764831845f);
            }
            float mx = fmx(fmx(sc[0], sc[1]), fmx(sc[2], sc[3]));
            mx = fmx(mx, dpp<XOR1>(mx)); mx = swap16_max(mx); mx = swap32_max(mx);
            float pe[4]; v4u pa;
#pragma unroll
            for (int kg = 0; kg < 4; ++kg) { pe[kg] = __expf(sc[kg] - mx); pa[kg] = pk_bf(0.f, pe[kg]) >> (odd ? 0 : 16); }
            float ls = (pe[0] + pe[1]) + (pe[2] + pe[3]);
            ls += dpp<XOR1>(ls); ls = swap16_add(ls, ls); ls = swap32_add(ls, ls);
            if (lane < 16 && !odd) *(v2f*)&L.ml[n >> 1][a][0] = v2f{mx, ls};
            ST(5);
            v4f oa[2], ob[2];
#pragma unroll
            for (int d = 0; d < 8; ++d) {
                v4u vt;
#pragma unroll
                for (int e = 0; e < 4; ++e) vt[e] = __builtin_amdgcn_perm(vr[2 * e + 1][d >> 1], vr[2 * e][d >> 1], (d & 1) ? 0x07060302u : 0x05040100u);
                const v4f r = mfma(pa, vt, v4f{0, 0, 0, 0});
                oa[d >> 2][d & 3] = r[0] + r[1]; ob[d >> 2][d & 3] = r[2] + r[3];
            }
            float* ow = L.ow[a] + g * 2 * OWS + n * 4;
            *(v4f*)(ow) = oa[0]; *(v4f*)(ow + 64) = oa[1]; *(v4f*)(ow + OWS) = ob[0]; *(v4f*)(ow + OWS + 64) = ob[1];
            lds_add<C_M>(1);
            ST(6);
        }
        {
            if (DLO && blk % 37 == 5) until<0>(t0 + DLO * 16);
            lds_wait<0>(C_M * 4, 4);
            const v4f m0 = *(const v4f*)&L.ml[hd][0][0], m1 = *(const v4f*)&L.ml[hd][2][0];
            v4f o4[4];
#pragma unroll
            for (int w = 0; w < 4; ++w) o4[w] = *(const v4f*)(L.ow[w] + hd * OWS + (ln & 1) * 64 + (ln >> 1) * 4);
            const float M = fmx(fmx(m0[0], m0[2]), fmx(m1[0], m1[2]));
            const float c0 = __expf(m0[0] - M), c1 = __expf(m0[2] - M), c2 = __expf(m1[0] - M), c3 = __expf(m1[2] - M);
            const float Ls = (m0[1] * c0 + m0[3] * c1) + (m1[1] * c2 + m1[3] * c3);
            const v4f o = ((o4[0] * c0 + o4[1] * c1) + (o4[2] * c2 + o4[3] * c3)) * __builtin_amdgcn_rcpf(Ls);
            const unsigned lse = f2u(M + __logf(Ls)), p0 = pk_bf(o[0], o[1]), p1 = pk_bf(o[2], o[3]);
            const unsigned e0 = __builtin_amdgcn_perm(lse, T, 0x05010400u), e1 = __builtin_amdgcn_perm(lse, T, 0x07030602u);
            st4(rw, so, v4u{__builtin_amdgcn_perm(p0, e0, 0x05040100u), __builtin_amdgcn_perm(p0, e0, 0x07060302u),
                            __builtin_amdgcn_perm(p1, e1, 0x05040100u), __builtin_amdgcn_perm(p1, e1, 0x07060302u)});
            const int tg = later(clk() + GO * 16, t0 + TO * 16);
            ST(7);
            until<1>(tg);
            ST(8);
        }
        {
            v4u d[4];
            const int it = gather(d, T, rw, rs, oo);
            ST(9);
            float l[4];
#pragma unroll
            for (int i = 0; i < 4; ++i)
                l[i] = u2f(__builtin_amdgcn_perm(__builtin_amdgcn_perm(d[i][3], d[i][2], 0x0501u), __builtin_amdgcn_perm(d[i][1], d[i][0], 0x0501u), 0x05040100u));
            float M = fmx(fmx(l[0], l[1]), fmx(l[2], l[3]));
            M = fmx(M, dpp<ROR8>(M)); M = swap16_max(M); M = swap32_max(M);
            v4f acc = {0, 0, 0, 0};
#pragma unroll
            for (int i = 0; i < 4; ++i) { l[i] = __expf(l[i] - M); acc += __builtin_bit_cast(v4f, d[i] & 0xffff0000u) * l[i]; }
            float Ls = (l[0] + l[1]) + (l[2] + l[3]);
            Ls += dpp<ROR8>(Ls); Ls = swap16_add(Ls, Ls); Ls = swap32_add(Ls, Ls);
            float r = swap16_add(swap32_add(acc[0], acc[2]), swap32_add(acc[1], acc[3]));
            r += dpp<ROR8>(r);
            r *= __builtin_amdgcn_rcpf(Ls);
            L.tab[TAB_O + ((a >> 1) * 4 + g) * TS + (hd ^ wr) * 2 + (a & 1)] = pk_bf(r, r);
            lds_add<C_O>(1);
            ST(10); STV(15, it);
        }
        asm volatile("" :: "v"(pf));
    }
    if (!heavy && wave >= 2 && wave < FW) {
        const int oct = (j & 3) * 2 + (wave & 1);
        const int ocol = (oct >> 2) * 1024 + x * 128 + (oct & 1) * 64 + (lane & 7) * 8 + (oct & 2) * 2 + g;
        const int lo = WS_FP + (x * 8 + oct) * FPS + lane * 16, l2 = lane < 32 ? lo : lo - 512;
        const unsigned xo = __builtin_amdgcn_raw_buffer_load_b16(mk(xp, 4096), ocol * 2, 0, 0);
        asm volatile("" :: "v"(xo), "v"(l2));
        until<1>(t0 + TF * 16 + ((wave - 2) >> 1) * (FST * 16));
        ST(11);
        v4u d[12];
        auto ld = [&](int i) { return ld4<SC1>(rw, (i < 11 ? lo : l2) + (i & 3) * 1024, (i >> 2) * 4096); };
#pragma unroll
        for (int i = 0; i < 12; ++i) d[i] = ld(i);
        int it = 0; bool mine = true;
#pragma unroll 1
        for (;; ++it) {
            bool bad[12], more = false;
#pragma unroll
            for (int i = 0; i < 12; ++i) { bad[i] = tag_of(d[i]) != T; more |= bad[i]; }
            if (FW > 4 && lds_get(ctl + C_F + (wave & 1))) { mine = false; break; }
            if (!any(more) || it >= SPIN) break;
            CB();
#pragma unroll
            for (int i = 0; i < 12; ++i) if (bad[i]) d[i] = ld(i);
        }
        if (mine) {
            if (lane == 0) ctl[C_F + (wave & 1)] = 1;
            ST(12);
            const v4f s4 = ((asf(d[0]) + asf(d[1])) + (asf(d[2]) + asf(d[3]))) + ((asf(d[4]) + asf(d[5])) + (asf(d[6]) + asf(d[7])));
            const v4f f4 = (asf(d[8]) + asf(d[9])) + (asf(d[10]) + (lane < 32 ? asf(d[11]) : v4f{0, 0, 0, 0}));
            float s = swap16_add(swap32_add(s4[0], s4[2]), swap32_add(s4[1], s4[3]));
            float f = swap16_add(swap32_add(f4[0], f4[2]), swap32_add(f4[1], f4[3]));
            s = rbf(s + dpp<ROR8>(s));
            f = rbf(f + dpp<ROR8>(f));
            const float ga = rbf(f / (1.f + __expf(-f)));
            __builtin_amdgcn_raw_buffer_store_b16((unsigned short)pk_bf(blo(xo) + rbf(s * ga), 0.f), mk(outp, 4096), (lane & 8) ? OOB : ocol * 2, 0, 0);
            if (blk == 255) st1(rw, lane == 0 ? WS_EP : OOB, T);
        }
        ST(13); STV(3, it);
    }
}

extern "C" void kernel_launch(void* const* d_in, const int* in_sizes, int n_in, void* d_out, int out_size, void* d_ws, size_t ws_size,
                              hipStream_t stream) {
    fused_decode<<<256, 512, 0, stream>>>(d_in[0], d_in[1], d_in[2], d_in[3], d_in[4], d_in[6], d_in[7], d_out, d_ws);
}
```
